```python
import torch

def _softmax(x, axis=-1):
    return torch.softmax(x, dim=axis)
def _gelu(x):
    return torch.nn.functional.gelu(x, approximate="tanh")
def _silu(x):
    return torch.nn.functional.silu(x)
def _sigmoid(x):
    return torch.sigmoid(x)
def _layernorm(x, axis=-1, eps=1e-5):
    m = x.mean(dim=axis, keepdim=True)
    v = x.var(dim=axis, keepdim=True, unbiased=False)
    return (x - m) / torch.sqrt(v + eps)
def _scatter_add(tgt, idx, src, axis=0):
    return tgt.clone().index_add_(axis, idx, src)
def _segment_sum(src, seg, n):
    out = torch.zeros((n,) + src.shape[1:], dtype=src.dtype)
    return out.index_add_(0, seg, src)
def _pad(x, pw):
    flat = tuple(v for pair in reversed(pw) for v in pair)
    return torch.nn.functional.pad(x, flat)

kernel_name = 'parallel_sum__ssm_conv_s4__dense_activation_mish__prompt'

def setup_inputs():

    _w = (lambda a: (lambda t: t.to(torch.bfloat16) if t.is_floating_point() else t)(torch.as_tensor(a)))
    def _A_setup():
        B, L, D, N = 1, 2048, 512, 128
        A_re = -(lambda _x: torch.exp(torch.as_tensor(_x)))((lambda *s: torch.randn(*s, dtype=torch.bfloat16) * 6.4)(D, N))
        A_im = torch.pi * (lambda *s: torch.randn(*s, dtype=torch.bfloat16) * 0.05)(D, N)
        B_re = (lambda *s: torch.randn(*s, dtype=torch.bfloat16) * 0.1)(D, N)
        B_im = (lambda *s: torch.randn(*s, dtype=torch.bfloat16) * 0.1)(D, N)
        C_re = (lambda *s: torch.randn(*s, dtype=torch.bfloat16) * 0.1)(D, N)
        C_im = (lambda *s: torch.randn(*s, dtype=torch.bfloat16) * 0.1)(D, N)
        log_dt = (lambda *s: torch.randn(*s, dtype=torch.bfloat16) * 0.05)(D) * 0.5 - 4.0
        Dskip = (lambda *s: torch.randn(*s, dtype=torch.bfloat16) * 0.05)(D)
        return {
            "u": _w((lambda *s: torch.randn(*s, dtype=torch.bfloat16) * 0.05)(B, L, D)),
            "A_re": _w(A_re),
            "A_im": _w(A_im),
            "B_re": _w(B_re),
            "B_im": _w(B_im),
            "C_re": _w(C_re),
            "C_im": _w(C_im),
            "log_dt": _w(log_dt),
            "Dskip": _w(Dskip),
        }
    _A_in = _A_setup()

    def _B_setup():
        B, S, D, F = 1, 2048, 512, 1792
        return {
            "x": _w((lambda *s: torch.randn(*s, dtype=torch.bfloat16) * 0.05)(B, S, D)),
            "w1": _w((lambda *s: torch.randn(*s, dtype=torch.bfloat16) * 0.8)(D, F) * (D ** -0.5)),
            "b1": _w((lambda *s: torch.randn(*s, dtype=torch.bfloat16) * 0.2)(F)),
            "w2": _w((lambda *s: torch.randn(*s, dtype=torch.bfloat16) * 0.2)(F, D) * (F ** -0.5)),
        }
    _B_in = _B_setup()

    return {
        "A_u": _A_in["u"],
        "A_A_re": _A_in["A_re"],
        "A_A_im": _A_in["A_im"],
        "A_B_re": _A_in["B_re"],
        "A_B_im": _A_in["B_im"],
        "A_C_re": _A_in["C_re"],
        "A_C_im": _A_in["C_im"],
        "A_log_dt": _A_in["log_dt"],
        "A_Dskip": _A_in["Dskip"],
        "B_x": _B_in["x"],
        "B_w1": _B_in["w1"],
        "B_b1": _B_in["b1"],
        "B_w2": _B_in["w2"],
    }

def reference(A_u, A_A_re, A_A_im, A_B_re, A_B_im, A_C_re, A_C_im, A_log_dt, A_Dskip, B_x, B_w1, B_b1, B_w2):
    def _A_ref(u, A_re, A_im, B_re, B_im, C_re, C_im, log_dt, Dskip):
        Bsz, L, D = u.shape
        dt = torch.exp(log_dt)[:, None]
        A = A_re + 1j * A_im
        Bc = B_re + 1j * B_im
        Cc = C_re + 1j * C_im
        logdA = dt * A
        dB = dt * Bc
        l = (lambda x, dt: x.to(dt))(torch.arange(L), A_re.dtype)
        P = torch.exp(logdA[:, :, None] * l[None, None, :])
        K = torch.einsum("dn,dnl,dn->dl", Cc, P, dB).real
        L2 = 2 * L
        Kp = _pad(K, ((0, 0), (0, L)))
        up = (lambda x, *d: x.permute(*d))(u, 0, 2, 1)
        up = _pad(up, ((0, 0), (0, 0), (0, L)))
        Kf = (lambda x, axes=(-2,-1): torch.fft.rfft2(x.float(), dim=axes))(Kp[:, None, :], axes=(-2, -1))
        Uf = (lambda x, axes=(-2,-1): torch.fft.rfft2(x.float(), dim=axes))(up[:, :, None, :], axes=(-2, -1))
        Yf = Uf * Kf[None, :, :, :]
        y = (lambda x, s, axes=(-2,-1): torch.fft.irfft2(x, s=s, dim=axes).to(torch.bfloat16))(Yf, (1, L2), axes=(-2, -1))[:, :, 0, :L]
        y = (lambda x, *d: x.permute(*d))(y, 0, 2, 1) + u * Dskip[None, None, :]
        return y

    def _B_ref(x, w1, b1, w2):
        h = torch.einsum("bsd,df->bsf", x, w1) + b1
        sp = torch.log1p(torch.exp(-torch.abs(h))) + torch.where(h > 0, h, torch.zeros_like(h))
        act = h * torch.tanh(sp)
        return torch.einsum("bsf,fd->bsd", act, w2)

    x = A_u
    xn = _layernorm(x, axis=-1)
    return x + _A_ref(xn, A_A_re, A_A_im, A_B_re, A_B_im, A_C_re, A_C_im, A_log_dt, A_Dskip) + _B_ref(xn, B_w1, B_b1, B_w2)
```

```cpp
#include <hip/hip_runtime.h>
#include <hip/hip_bf16.h>
#include <stdint.h>

typedef unsigned short u16;
static __device__ __forceinline__ float bf2f(u16 v){ return __uint_as_float(((unsigned)v)<<16); }
static __device__ __forceinline__ u16 f2bf(float f){ __hip_bfloat16 b = __float2bfloat16(f); return *(u16*)&b; }
static __device__ __forceinline__ float rbf(float f){ return bf2f(f2bf(f)); }

constexpr int L=2048, D=512, N=128, F=1792;

__global__ void k_ln(const u16* x, u16* xn){
  int t = blockIdx.x; int lane = threadIdx.x;
  float v[8]; float s=0;
  for(int i=0;i<8;i++){ v[i]=bf2f(x[t*D+lane*8+i]); s+=v[i]; }
  for(int o=32;o>0;o>>=1) s+=__shfl_xor(s,o);
  float m=s/D; float q=0;
  for(int i=0;i<8;i++){ float d=v[i]-m; q+=d*d; }
  for(int o=32;o>0;o>>=1) q+=__shfl_xor(q,o);
  float var=rbf(q/D); float mb=rbf(m);
  float sd=rbf(sqrtf(rbf(var+1e-5f)));
  for(int i=0;i<8;i++){ xn[t*D+lane*8+i]=f2bf(rbf(v[i]-mb)/sd); }
}
__device__ int lround_bf(int l){ if(l<256) return l; int k=31-__clz(l); int sh=k-7; return ((l+(1<<(sh-1))-1+((l>>sh)&1))>>sh)<<sh; }
__global__ void k_kgen(const u16*Are,const u16*Aim,const u16*Bre,const u16*Bim,const u16*Cre,const u16*Cim,const u16*ldt,float*K){
  int d=blockIdx.x;
  float dt=rbf(expf(bf2f(ldt[d])));
  for(int l=threadIdx.x;l<L;l+=blockDim.x){
    float lf=(float)lround_bf(l);
    float acc=0;
    for(int n=0;n<N;n++){
      float a=dt*bf2f(Are[d*N+n]), b=dt*bf2f(Aim[d*N+n]);
      float br=dt*bf2f(Bre[d*N+n]), bi=dt*bf2f(Bim[d*N+n]);
      float cr=bf2f(Cre[d*N+n]), ci=bf2f(Cim[d*N+n]);
      float wr=cr*br-ci*bi, wi=cr*bi+ci*br;
      float e=expf(a*lf); float s,c; sincosf(b*lf,&s,&c);
      acc+=e*(wr*c-wi*s);
    }
    K[d*L+l]=acc;
  }
}
__global__ void k_conv(const u16*xn,const float*K,float*y){
  __shared__ float su[L]; __shared__ float sk[L];
  int d=blockIdx.x;
  for(int t=threadIdx.x;t<L;t+=blockDim.x){ su[t]=bf2f(xn[t*D+d]); sk[t]=K[d*L+t]; }
  __syncthreads();
  for(int t=threadIdx.x;t<L;t+=blockDim.x){
    float acc=0; for(int s=0;s<=t;s++) acc+=sk[s]*su[t-s];
    y[t*D+d]=acc;
  }
}
__global__ void k_gemm1(const u16*xn,const u16*w1,const u16*b1,u16*act){
  int t=blockIdx.x; __shared__ float sx[D];
  for(int i=threadIdx.x;i<D;i+=blockDim.x) sx[i]=bf2f(xn[t*D+i]);
  __syncthreads();
  for(int f=threadIdx.x;f<F;f+=blockDim.x){
    float acc=0; for(int k=0;k<D;k++) acc+=sx[k]*bf2f(w1[k*F+f]);
    float h=acc+bf2f(b1[f]);
    float e=expf(fminf(h,30.f)); float n=e*(e+2.f);
    act[t*F+f]=f2bf(h*n/(n+2.f));
  }
}
__global__ void k_gemm2(const u16*act,const u16*w2,const u16*x,const u16*xn,const float*y,const u16*Dsk,u16*out){
  int t=blockIdx.x; __shared__ float sa[F];
  for(int i=threadIdx.x;i<F;i+=blockDim.x) sa[i]=bf2f(act[t*F+i]);
  __syncthreads();
  for(int d=threadIdx.x;d<D;d+=blockDim.x){
    float acc=0; for(int k=0;k<F;k++) acc+=sa[k]*bf2f(w2[k*D+d]);
    float ya=rbf(rbf(y[t*D+d])+rbf(bf2f(xn[t*D+d])*bf2f(Dsk[d])));
    out[t*D+d]=f2bf(rbf(bf2f(x[t*D+d])+ya)+rbf(acc));
  }
}
extern "C" void kernel_launch(void* const* d_in, const int* in_sizes, int n_in,
                              void* d_out, int out_size, void* d_ws, size_t ws_size,
                              hipStream_t stream) {
  const u16* x=(const u16*)d_in[0];
  char* ws=(char*)d_ws;
  u16* xn=(u16*)ws; float* K=(float*)(ws+(4<<20)); float* y=(float*)(ws+(12<<20)); u16* act=(u16*)(ws+(20<<20));
  k_ln<<<L,64,0,stream>>>(x,xn);
  k_kgen<<<D,256,0,stream>>>((const u16*)d_in[1],(const u16*)d_in[2],(const u16*)d_in[3],(const u16*)d_in[4],(const u16*)d_in[5],(const u16*)d_in[6],(const u16*)d_in[7],K);
  k_conv<<<D,256,0,stream>>>(xn,K,y);
  k_gemm1<<<L,256,0,stream>>>(xn,(const u16*)d_in[10],(const u16*)d_in[11],act);
  k_gemm2<<<L,256,0,stream>>>(act,(const u16*)d_in[12],x,xn,y,(const u16*)d_in[8],(u16*)d_out);
}
```

```cpp
#include <hip/hip_runtime.h>
#include <stdint.h>
#include <type_traits>

typedef unsigned short u16;
typedef unsigned int u32;
typedef unsigned long long u64;
typedef __attribute__((ext_vector_type(8))) __bf16 bf16x8;
typedef __attribute__((ext_vector_type(2))) __bf16 bf16x2;
typedef __attribute__((ext_vector_type(8))) _Float16 f16x8;
typedef __attribute__((ext_vector_type(4))) _Float16 f16x4;
typedef __attribute__((ext_vector_type(2))) _Float16 f16x2;
typedef __attribute__((ext_vector_type(2))) float f32x2;
typedef __attribute__((ext_vector_type(4))) float f32x4;
typedef __attribute__((ext_vector_type(16))) float f32x16;
typedef __attribute__((ext_vector_type(4))) u32 u32x4;
typedef __attribute__((ext_vector_type(2))) u32 u32x2;
typedef __attribute__((ext_vector_type(4))) short s16x4;
#define AS3 __attribute__((address_space(3)))
#define DEVI __device__ __forceinline__

constexpr size_t WS_STAT = 0, WS_CS = 16384, WS_UFLAG = 30720, WS_YFLAG = 31744  , WS_HFLAG = 35840  ,
                 WS_SYNC_BYTES = 39936;
constexpr size_t WS_U = 1u << 20, WS_Y = 4u << 20, WS_H = 8u << 20, WS_DBG = 32u << 20;
constexpr u32 L_LNS = 65536;
constexpr u32 L_KREG = 98304;
constexpr u32 L_UL = L_KREG;
constexpr u32 L_KCP = L_KREG + 18432;
constexpr u32 L_EV = L_KCP + 33280;
constexpr u32 L_SPARE = L_KREG + 49152;
constexpr u32 SPIN_MAX = 1u << 15;

template <typename T> DEVI T ldsld(u32 a) { return *(AS3 T*)(uintptr_t)a; }
template <typename T> DEVI void ldsst(u32 a, T v) { *(AS3 T*)(uintptr_t)a = v; }
DEVI u32x2 ldstr(u32 a) { return __builtin_bit_cast(u32x2, __builtin_amdgcn_ds_read_tr16_b64_v4i16((AS3 s16x4*)(uintptr_t)a)); }
DEVI float bf2f(u32 v) { return __uint_as_float(v << 16); }
DEVI float bfhi(u32 v) { return __uint_as_float(v & 0xffff0000u); }
DEVI float rbf(float f) { return (float)(__bf16)f; }
DEVI u32 pkbf(float a, float b) { bf16x2 v = {(__bf16)a, (__bf16)b}; return __builtin_bit_cast(u32, v); }
DEVI u32 pkh(float a, float b) { f16x2 v = {(_Float16)a, (_Float16)b}; return __builtin_bit_cast(u32, v); }

template <int STEP, bool COH>
DEVI void glds4(const void* sbase, u32 v0, u32 v1, u32 v2, u32 v3, u32 lds0) {
  u32 keep;
  if (COH)
    asm volatile(
        "s_mov_b32 %0, m0\n\ts_mov_b32 m0, %6\n\ts_nop 0\n\tglobal_load_lds_dwordx4 %1, %5 sc1\n\t"
        "s_add_u32 m0, m0, %7\n\ts_nop 0\n\tglobal_load_lds_dwordx4 %2, %5 sc1\n\t"
        "s_add_u32 m0, m0, %7\n\ts_nop 0\n\tglobal_load_lds_dwordx4 %3, %5 sc1\n\t"
        "s_add_u32 m0, m0, %7\n\ts_nop 0\n\tglobal_load_lds_dwordx4 %4, %5 sc1\n\ts_mov_b32 m0, %0"
        : "=&s"(keep) : "v"(v0), "v"(v1), "v"(v2), "v"(v3), "s"(sbase), "s"(lds0), "n"(STEP) : "memory", "scc");
  else
    asm volatile(
        "s_mov_b32 %0, m0\n\ts_mov_b32 m0, %6\n\ts_nop 0\n\tglobal_load_lds_dwordx4 %1, %5\n\t"
        "s_add_u32 m0, m0, %7\n\ts_nop 0\n\tglobal_load_lds_dwordx4 %2, %5\n\t"
        "s_add_u32 m0, m0, %7\n\ts_nop 0\n\tglobal_load_lds_dwordx4 %3, %5\n\t"
        "s_add_u32 m0, m0, %7\n\ts_nop 0\n\tglobal_load_lds_dwordx4 %4, %5\n\ts_mov_b32 m0, %0"
        : "=&s"(keep) : "v"(v0), "v"(v1), "v"(v2), "v"(v3), "s"(sbase), "s"(lds0), "n"(STEP) : "memory", "scc");
}
DEVI void glds1c(const void* sbase, u32 voff, u32 ldsaddr) {
  u32 keep;
  asm volatile("s_mov_b32 %0, m0\n\ts_mov_b32 m0, %3\n\ts_nop 0\n\tglobal_load_lds_dwordx4 %1, %2 sc1\n\ts_mov_b32 m0, %0"
               : "=&s"(keep) : "v"(voff), "s"(sbase), "s"(ldsaddr) : "memory");
}
DEVI void st16c(void* sbase, u32 voff, u32x4 v) {
  asm volatile("global_store_dwordx4 %0, %1, %2 sc1\n\ts_nop 1" ::"v"(voff), "v"(v), "s"(sbase) : "memory");
}
DEVI void st16(void* sbase, u32 voff, u32x4 v) {
  asm volatile("global_store_dwordx4 %0, %1, %2\n\ts_nop 1" ::"v"(voff), "v"(v), "s"(sbase) : "memory");
}
DEVI void st8c(void* sbase, u32 voff, u32x2 v) {
  asm volatile("global_store_dwordx2 %0, %1, %2 sc1\n\ts_nop 1" ::"v"(voff), "v"(v), "s"(sbase) : "memory");
}
DEVI void st4c(void* sbase, u32 voff, u32 v) {
  asm volatile("global_store_dword %0, %1, %2 sc1\n\ts_nop 1" ::"v"(voff), "v"(v), "s"(sbase) : "memory");
}
DEVI u32x2 ld8c_wait(const void* sbase, u32 voff) {
  u32x2 r;
  asm volatile("global_load_dwordx2 %0, %1, %2 sc1\n\ts_waitcnt vmcnt(0)" : "=&v"(r) : "v"(voff), "s"(sbase) : "memory");
  return r;
}
DEVI u32 ld4c_wait(const void* sbase, u32 voff) {
  u32 r;
  asm volatile("global_load_dword %0, %1, %2 sc1\n\ts_waitcnt vmcnt(0)" : "=&v"(r) : "v"(voff), "s"(sbase) : "memory");
  return r;
}
DEVI u32x4 ld16c_wait(const void* sbase, u32 voff) {
  u32x4 r;
  asm volatile("global_load_dwordx4 %0, %1, %2 sc1\n\ts_waitcnt vmcnt(0)" : "=&v"(r) : "v"(voff), "s"(sbase) : "memory");
  return r;
}
template <int N> DEVI void waitv() { asm volatile("s_waitcnt vmcnt(%0)" ::"n"(N) : "memory"); }
DEVI void bar() {
  asm volatile("s_waitcnt lgkmcnt(0)" ::: "memory");
  __builtin_amdgcn_s_barrier();
  asm volatile("" ::: "memory");
}
template <int CTRL, int RM> DEVI float dppf(float old, float v) {
  return __int_as_float(__builtin_amdgcn_update_dpp(__float_as_int(old), __float_as_int(v), CTRL, RM, 0xf, false));
}
DEVI float wave_sum(float v) {
  v += dppf<0x111, 0xf>(0.f, v); v += dppf<0x112, 0xf>(0.f, v); v += dppf<0x114, 0xf>(0.f, v); v += dppf<0x118, 0xf>(0.f, v);
  v += dppf<0x142, 0xa>(0.f, v); v += dppf<0x143, 0xc>(0.f, v);
  return __int_as_float(__builtin_amdgcn_readlane(__float_as_int(v), 63));
}
DEVI float wave_max0(float v) {
  v = fmaxf(v, dppf<0x111, 0xf>(0.f, v)); v = fmaxf(v, dppf<0x112, 0xf>(0.f, v)); v = fmaxf(v, dppf<0x114, 0xf>(0.f, v));
  v = fmaxf(v, dppf<0x118, 0xf>(0.f, v)); v = fmaxf(v, dppf<0x142, 0xa>(0.f, v)); v = fmaxf(v, dppf<0x143, 0xc>(0.f, v));
  return __int_as_float(__builtin_amdgcn_readlane(__float_as_int(v), 63));
}
DEVI f32x2 cmul(f32x2 a, f32x2 b) {
  f32x2 t, r;
  asm("v_pk_mul_f32 %0, %1, %2 op_sel_hi:[0,1]" : "=v"(t) : "v"(a), "v"(b));
  asm("v_pk_fma_f32 %0, %1, %2, %3 op_sel:[1,1,0] op_sel_hi:[1,0,1] neg_lo:[1,0,0]" : "=v"(r) : "v"(a), "v"(b), "v"(t));
  return r;
}
DEVI f32x2 cpow(float a2, float fhi, float flo, float R) {
  float m = __builtin_amdgcn_exp2f(a2 * R);
  float ph = __builtin_fmaf(flo, R, __builtin_amdgcn_fractf(fhi * R));
  return (f32x2){m * __builtin_amdgcn_cosf(ph), m * __builtin_amdgcn_sinf(ph)};
}
DEVI u32 kidx_exact(int l) {
  u32 u = __float_as_uint((float)l) >> 16;
  return l < 128 ? (u32)l : u - 0x4300u + 128u;
}
DEVI void pow2scale(float mx, int target, float& s, float& inv) {
  int ex = (int)((__float_as_uint(mx) >> 23) & 0xff);
  int se = 254 + target - ex;
  se = se < 2 ? 2 : (se > 252 ? 252 : se);
  s = __uint_as_float((u32)se << 23);
  inv = __uint_as_float((u32)(254 - se) << 23);
}
constexpr int g2_ops(int j) { return (j + 3 <= 13 ? 4 : 0) + (j + 4 <= 13 ? 4 : 0) + (j == 5 ? 1 : 0) + (j == 9 ? 2 : 0); }
constexpr int g2_wait(int s) {
  if (s < 4) { int n = (3 - s) * 4; for (int j = 1; j <= s - 1; j++) n += g2_ops(j); return n; }
  return (g2_ops(s - 3) - 4) + g2_ops(s - 2) + g2_ops(s - 1);
}
constexpr u32 hslot(int s) { return (s & 3) == 0 ? 81920u : L_KREG + (u32)((s & 3) - 1) * 16384u; }
constexpr u32 wslot(int s) { return (u32)(s % 5) * 16384u; }

template <int SET>
DEVI void conv_body(f32x4 (&acc)[4], u32 ub, u32 ab) {
  constexpr int A0 = SET ? 1 : 0, A1 = SET ? 2 : 3, A2 = SET ? 5 : 4, A3 = SET ? 6 : 7;
  f16x8 Bq[8];
#pragma unroll
  for (int q = 0; q <= A3; q++) Bq[q] = ldsld<f16x8>(ub + q * 512);
#pragma unroll
  for (int i = 0; i <= A3; i++) {
    u32x2 a0 = *(volatile AS3 u32x2*)(uintptr_t)(ab + (7 - i) * 512);
    u32x2 a1 = *(volatile AS3 u32x2*)(uintptr_t)(ab + (7 - i) * 512 + 8);
    f16x8 A = __builtin_bit_cast(f16x8, (u32x4){a0[0], a0[1], a1[0], a1[1]});
    if (A3 >= i) acc[3] = __builtin_amdgcn_mfma_f32_16x16x32_f16(A, Bq[A3 - i], acc[3], 0, 0, 0);
    if (A2 >= i) acc[2] = __builtin_amdgcn_mfma_f32_16x16x32_f16(A, Bq[A2 >= i ? A2 - i : 0], acc[2], 0, 0, 0);
    if (A1 >= i) acc[1] = __builtin_amdgcn_mfma_f32_16x16x32_f16(A, Bq[A1 >= i ? A1 - i : 0], acc[1], 0, 0, 0);
    if (A0 >= i) acc[0] = __builtin_amdgcn_mfma_f32_16x16x32_f16(A, Bq[A0 >= i ? A0 - i : 0], acc[0], 0, 0, 0);
  }
}

__global__ __launch_bounds__(512, 2) void fused_kernel(
    const u16* __restrict__ x, const u16* __restrict__ Are, const u16* __restrict__ Aim, const u16* __restrict__ Bre,
    const u16* __restrict__ Bim, const u16* __restrict__ Cre, const u16* __restrict__ Cim, const u16* __restrict__ ldt,
    const u16* __restrict__ Dsk, const u16* __restrict__ w1, const u16* __restrict__ b1, const u16* __restrict__ w2,
    u16* __restrict__ out, char* __restrict__ ws) {
  __shared__ __attribute__((aligned(16))) char sm[163840];
  asm volatile("" ::"v"(sm) : "memory");
  const u32 EP = 1u;
  const int tid = threadIdx.x, lane = tid & 63;
  const int w = __builtin_amdgcn_readfirstlane(tid >> 6);
  const int b = blockIdx.x, xcd = b & 7, bi = b >> 3;
  const bool isg1 = bi < 28;
  const int bm = 2 * xcd + (bi & 1), bn = bi >> 1;
  const int tb = 32 * xcd + bi;
  const int rot1 = (2 * bm + bn) & 7;
  const int bm2 = 4 * xcd + (bi & 3), bn2 = bi >> 2;
  const int rot2 = (bi * 5) % 14;
  const int wr = w & 3;
#define STAMP(i)
  STAMP(0);
  const int kch = w >> 2, kgrp = (w >> 1) & 1, kmh = w & 1;
  const int dch = 2 * tb + kch;
  u32x4 xr;
  u32 pa, pb, pc_, pd, pe, pf, pl;
  {
    u32 vo = (u32)((8 * tb + w) * 1024 + lane * 16);
    u32 po = (u32)((dch * 128 + kmh * 64 + lane) * 2);
    u32 lo = (u32)(dch * 2);
    asm volatile(
        "global_load_dwordx4 %0, %8, %11\n\t"
        "global_load_ushort %1, %9, %12\n\t"
        "global_load_ushort %2, %9, %13\n\t"
        "global_load_ushort %3, %9, %14\n\t"
        "global_load_ushort %4, %9, %15\n\t"
        "global_load_ushort %5, %9, %16\n\t"
        "global_load_ushort %6, %9, %17\n\t"
        "global_load_ushort %7, %10, %18"
        : "=&v"(xr), "=&v"(pa), "=&v"(pb), "=&v"(pc_), "=&v"(pd), "=&v"(pe), "=&v"(pf), "=&v"(pl)
        : "v"(vo), "v"(po), "v"(lo), "s"(x), "s"(Are), "s"(Aim), "s"(Bre), "s"(Bim), "s"(Cre), "s"(Cim), "s"(ldt)
        : "memory");
  }
  __builtin_amdgcn_s_barrier();
  u32 voX, voW;
  {
    int row = 32 * wr + (lane >> 3);
    voX = (u32)(row * 1024 + (((lane & 7) ^ ((row >> 1) & 7)) << 4));
    int k = 16 * wr + (lane >> 4);
    voW = (u32)(k * 3584 + (((lane & 15) ^ (((k & 1) << 1) | ((k & 2) << 2))) << 4));
  }
  const char* xb = (const char*)x + (size_t)bm * 131072;
  const char* wb = (const char*)w1 + bn * 256;
  auto issue1 = [&](int s) {
    int kt = (s + rot1) & 7;
    u32 st = (u32)((s % 3) * 32768 + wr * 4096);
    glds4<1024, false>(xb + kt * 128, voX, (voX ^ 64u) + 8192, voX + 16384, (voX ^ 64u) + 24576, st);
    glds4<1024, false>(wb + (size_t)kt * 229376, voW, voW + 14336, voW + 28672, voW + 43008, st + 16384);
  };
  const int ci = xcd * 4 + (bi - 28);
  if (w >= 4) {
    if (isg1) { issue1(0); issue1(1); waitv<16>(); }
    else waitv<0>();
  } else {
    if (isg1) waitv<0>();
    else {
      int cc = ci < 28 ? ci : 27;
      u32 vo = (u32)((8 * wr + (lane >> 3)) * 3584 + (lane & 7) * 16);
      const char* cb = (const char*)w1 + cc * 128;
#pragma unroll
      for (int j = 0; j < 4; j++)
        glds4<4096, false>(cb + (size_t)j * 128 * 3584, vo, vo + 32 * 3584, vo + 64 * 3584, vo + 96 * 3584, (u32)(j * 16384 + wr * 1024));
      waitv<16>();
    }
  }
  asm volatile("" : "+v"(xr), "+v"(pa), "+v"(pb), "+v"(pc_), "+v"(pd), "+v"(pe), "+v"(pf), "+v"(pl)::"memory");
  STAMP(1);
  {
    float v[8];
#pragma unroll
    for (int j = 0; j < 4; j++) { v[2 * j] = bf2f(xr[j]); v[2 * j + 1] = bfhi(xr[j]); }
    float s = ((v[0] + v[1]) + (v[2] + v[3])) + ((v[4] + v[5]) + (v[6] + v[7]));
    float mean = wave_sum(s) * (1.f / 512.f);
    float q = 0.f;
#pragma unroll
    for (int j = 0; j < 8; j++) { float d = v[j] - mean; q = __builtin_fmaf(d, d, q); }
    float var = wave_sum(q) * (1.f / 512.f);
    float mb = rbf(mean), sd = rbf(__builtin_sqrtf(rbf(rbf(var) + 1e-5f)));
    float rs = 1.f / sd;
    u32x4 o;
#pragma unroll
    for (int j = 0; j < 4; j++) {
      u32 dd = pkbf(v[2 * j] - mb, v[2 * j + 1] - mb);
      float d0 = bf2f(dd), d1 = bfhi(dd);
      float q0 = d0 * rs, q1 = d1 * rs;
      q0 = __builtin_fmaf(__builtin_fmaf(-q0, sd, d0), rs, q0);
      q1 = __builtin_fmaf(__builtin_fmaf(-q1, sd, d1), rs, q1);
      u32 qq = pkbf(q0, q1);
      o[j] = pkh(bf2f(qq), bfhi(qq));
    }
    ldsst<u32x4>(L_LNS + w * 1088 + lane * 16, o);
    bar();
    int li = lane & 15;
    u32 a = L_LNS + (li >> 2) * 1088 + ((tid & ~15) + 4 * (li & 3)) * 2;
    u32x2 p0 = ldstr(a), p1 = ldstr(a + 4 * 1088);
    st16c(ws + WS_U + (size_t)tb * 8192, (u32)tid * 16, (u32x4){p0[0], p0[1], p1[0], p1[1]});
    u32 pay = (__float_as_uint(mb) >> 16) | (__float_as_uint(sd) & 0xffff0000u);
    if (lane == 0) st8c(ws + WS_STAT + (size_t)(8 * tb + w) * 8, 0, (u32x2){pay, EP});
  }
  STAMP(2);
  {
    float dt = rbf(expf(bf2f(pl)));
    float a2 = (dt * bf2f(pa)) * 1.44269504088896341f;
    float bb = dt * bf2f(pb);
    f32x2 W = cmul((f32x2){bf2f(pe), bf2f(pf)}, (f32x2){dt * bf2f(pc_), dt * bf2f(pd)});
    const float C_HI = 0.15915494309189535f, C_LO = (float)(0.15915494309189533577 - (double)0.15915494309189535f);
    float p = bb * C_HI;
    float er = __builtin_fmaf(bb, C_HI, -p);
    float fhi = __uint_as_float(__float_as_uint(p) & 0xfffff000u);
    float flo = (p - fhi) + __builtin_fmaf(bb, C_LO, er);
    float g = 0.f;
    if (__any(a2 > 0.f)) { g = wave_max0(fmaxf(a2, 0.f)); a2 -= g; }
    float wsum = wave_sum(fabsf(W[0]) + fabsf(W[1]));
    float sig, isig;
    pow2scale(wsum, 13, sig, isig);
    const float cs = kgrp ? 8.f : 2.f, rs = kgrp ? 128.f : 32.f, rb = kgrp ? 512.f : 0.f;
    f32x2 zc = cpow(a2, fhi, flo, cs), zc4 = cpow(a2, fhi, flo, 4.f * cs);
    f32x2 zr = cpow(a2, fhi, flo, rs), zr4 = cpow(a2, fhi, flo, 4.f * rs);
    f32x2 zb = cpow(a2, fhi, flo, rb), zx = cpow(a2, fhi, flo, 0.5f * cs);
    zc[1] = -zc[1]; zc4[1] = -zc4[1];
    const u32 T = L_KREG + w * 8192;
    const u32 wbase = T + (lane >> 2) * 16 + (lane & 3) * 4;
    f32x2 c[4], r[8], rr4[4];
    c[0] = (f32x2){W[0] * sig, -W[1] * sig};
    c[1] = cmul(c[0], zc); c[2] = cmul(c[1], zc); c[3] = cmul(c[2], zc);
    rr4[0] = zb; rr4[1] = cmul(rr4[0], zr); rr4[2] = cmul(rr4[1], zr); rr4[3] = cmul(rr4[2], zr);
#pragma unroll
    for (int o = 0; o < 4; o++) {
#pragma unroll
      for (int j = 0; j < 4; j++) {
        int e = 4 * o + j;
        if (o > 0) { c[j] = cmul(c[j], zc4); rr4[j] = cmul(rr4[j], zr4); }
        if (o < 2) r[e] = rr4[j];
        u32 a = wbase ^ (u32)(e << 4);
        ldsst<u32>(a + e * 256, pkh(rr4[j][0], rr4[j][1]));
        ldsst<u32>(a + e * 256 + 4096, pkh(c[j][0], c[j][1]));
      }
    }
    const int rr = lane & 15, kg = lane >> 4;
    const u32 ra = T + rr * 256 + ((kg ^ rr) << 4);
    f16x8 bf[4];
    f32x4 d0 = {0.f, 0.f, 0.f, 0.f}, d1 = {0.f, 0.f, 0.f, 0.f};
#pragma unroll
    for (int s = 0; s < 4; s++) {
      f16x8 af = ldsld<f16x8>(ra ^ (u32)(s << 6));
      bf[s] = ldsld<f16x8>((ra ^ (u32)(s << 6)) + 4096);
      d0 = __builtin_amdgcn_mfma_f32_16x16x32_f16(af, bf[s], d0, 0, 0, 0);
    }
#pragma unroll
    for (int e = 0; e < 8; e++) {
      f32x2 t = cmul(r[e], zx);
      ldsst<u32>((wbase ^ (u32)(e << 4)) + e * 256, pkh(t[0], t[1]));
    }
#pragma unroll
    for (int s = 0; s < 4; s++) {
      f16x8 af = ldsld<f16x8>(ra ^ (u32)(s << 6));
      d1 = __builtin_amdgcn_mfma_f32_16x16x32_f16(af, bf[s], d1, 0, 0, 0);
    }
    const int sh = kgrp ? 2 : 0;
    const u32 P = T + 4096;
#pragma unroll
    for (int e = 0; e < 4; e++) {
      int l0 = (kgrp ? 512 : 0) + ((32 * (4 * kg + e) + 2 * rr) << sh);
      int l1 = l0 + (1 << sh);
      float s0 = isig, s1 = isig;
      if (g > 0.f) { s0 *= __builtin_amdgcn_exp2f(g * (float)l0); s1 *= __builtin_amdgcn_exp2f(g * (float)l1); }
      u32 i0 = l0 <= 2048 ? kidx_exact(l0) : 700u;
      u32 i1 = l1 < (256 << sh) ? kidx_exact(l1) : 701u;
      ldsst<float>(P + i0 * 4, d0[e] * s0);
      ldsst<float>(P + i1 * 4, d1[e] * s1);
    }
    if (lane == 0) ldsst<float>(P + 704 * 4, wsum * __builtin_amdgcn_exp2f(g * 2048.f));
  }
  STAMP(3);
  const int hh = lane >> 5, blk = (lane >> 4) & 1, q4 = (lane & 15) >> 2, p4 = lane & 3;
  if (w < 4) {
    __builtin_amdgcn_s_setprio(2);
    if (isg1) {
      const int wf = wr & 1, wt = wr >> 1;
      const int tl = wt * 64 + (lane & 31);
      const u32 xa0 = (u32)(tl * 128 + ((hh ^ ((tl >> 1) & 7)) << 4));
      const u32 wa0 = (u32)(16384 + (8 * hh + q4) * 256 +
                            (((8 * wf + 4 * (p4 & 1) + blk) ^ (((q4 & 1) << 1) | ((q4 & 2) << 2))) << 4) + 8 * (p4 >> 1));
      f32x16 acc[2][2];
#pragma unroll
      for (int i = 0; i < 16; i++) { acc[0][0][i] = 0.f; acc[0][1][i] = 0.f; acc[1][0][i] = 0.f; acc[1][1][i] = 0.f; }
      bf16x8 FX[2][4][2], FW[2][4][2];
      auto rdf = [&](int s, int ks) {
        const u32 sb = (u32)((s % 3) * 32768);
#pragma unroll
        for (int j = 0; j < 2; j++) FX[s & 1][ks][j] = ldsld<bf16x8>(sb + (xa0 ^ (u32)(ks << 5)) + j * 4096);
#pragma unroll
        for (int tau = 0; tau < 2; tau++) {
          u32 a = sb + (wa0 ^ (u32)(tau << 5)) + ks * 4096;
          u32x2 a0 = ldstr(a), a1 = ldstr(a + 1024);
          FW[s & 1][ks][tau] = __builtin_bit_cast(bf16x8, (u32x4){a0[0], a0[1], a1[0], a1[1]});
        }
      };
      bar();
      STAMP(4);
#pragma unroll
      for (int ks = 0; ks < 4; ks++) rdf(0, ks);
#pragma unroll
      for (int s = 0; s < 8; s++) {
        if (s == 0) waitv<0>();
        if (s == 0) STAMP(11);
        if (s == 1) STAMP(13);
        if (s == 5) STAMP(15);
        if (s == 7) STAMP(18);
        bar();
        if (s == 0) STAMP(12);
        if (s == 1) STAMP(14);
        if (s == 5) STAMP(16);
        if (s == 6) STAMP(17);
        if (s == 1 && tid == 0) st4c(ws + WS_UFLAG, (u32)tb * 4, EP);
#pragma unroll
        for (int ks = 0; ks < 4; ks++) {
          if (s < 7) rdf(s + 1, ks);
#pragma unroll
          for (int tau = 0; tau < 2; tau++)
#pragma unroll
            for (int j = 0; j < 2; j++)
              acc[tau][j] = __builtin_amdgcn_mfma_f32_32x32x16_bf16(FW[s & 1][ks][tau], FX[s & 1][ks][j], acc[tau][j], 0, 0, 0);
          __builtin_amdgcn_sched_barrier(0);
        }
      }
      STAMP(5);
      char* hp = ws + WS_H + ((size_t)bm * 224 + bn * 16 + wf * 8) * 2048;
#pragma unroll
      for (int j = 0; j < 2; j++) {
        f32x2 st = ldsld<f32x2>(L_EV + 1024 + (tl + 32 * j) * 8);
        const f32x2 r2 = {st[0], st[0]}, rm2 = {-st[1], -st[1]};
#pragma unroll
        for (int tau = 0; tau < 2; tau++) {
#pragma unroll
          for (int hf = 0; hf < 2; hf++) {
            u32x4 o;
#pragma unroll
            for (int jj = 0; jj < 4; jj++) {
              f32x4 cb = ldsld<f32x4>(L_EV + (wf * 32 + 16 * hh + 8 * tau + 4 * hf + jj) * 16);
              f32x2 av = {acc[tau][j][8 * hf + 2 * jj], acc[tau][j][8 * hf + 2 * jj + 1]};
              f32x2 h = __builtin_elementwise_fma(r2, av, __builtin_elementwise_fma(rm2, (f32x2){cb[0], cb[1]}, (f32x2){cb[2], cb[3]}));
              f32x2 h2 = h * 1.44269504088896341f;
              f32x2 g1 = (f32x2){__builtin_amdgcn_exp2f(h2[0]), __builtin_amdgcn_exp2f(h2[1])} + 1.f;
              f32x2 dn = __builtin_elementwise_fma(g1, g1, (f32x2){1.f, 1.f});
              f32x2 rc = {__builtin_amdgcn_rcpf(dn[0]), __builtin_amdgcn_rcpf(dn[1])};
              f32x2 ov = __builtin_elementwise_fma(h * rc, (f32x2){-2.f, -2.f}, h);
              o[jj] = pkbf(ov[0], ov[1]);
            }
            st16c(hp, (u32)((4 * hh + 2 * tau + hf) * 2048 + (tl + 32 * j) * 16), o);
          }
        }
      }
      STAMP(6);
      waitv<0>();
      if (lane == 0) st4c(ws + WS_HFLAG, (u32)((bm * 2 + wt) * 32 + bn * 2 + wf) * 4, EP);
      STAMP(7);
    } else {
      waitv<0>();
      float cs[8];
#pragma unroll
      for (int e = 0; e < 8; e++) cs[e] = 0.f;
#pragma unroll
      for (int j = 0; j < 16; j++) {
        u32x4 cw = ldsld<u32x4>((u32)(j * 4096 + tid * 16));
#pragma unroll
        for (int e = 0; e < 4; e++) { cs[2 * e] += bf2f(cw[e]); cs[2 * e + 1] += bfhi(cw[e]); }
      }
      ldsst<f32x4>((u32)(81920 + tid * 32), (f32x4){cs[0], cs[1], cs[2], cs[3]});
      ldsst<f32x4>((u32)(81920 + tid * 32 + 16), (f32x4){cs[4], cs[5], cs[6], cs[7]});
      bar();
      if (w == 0) {
        float s = 0.f;
#pragma unroll 8
        for (int r = 0; r < 32; r++) s += ldsld<float>((u32)(81920 + r * 256 + lane * 4));
        if (ci < 28) st8c(ws + WS_CS + (size_t)ci * 512, (u32)lane * 8, (u32x2){__float_as_uint(s), EP});
      }
#pragma unroll 1
      for (int s = 0; s < 8; s++) {
        bar();
        if (s == 1 && tid == 0) st4c(ws + WS_UFLAG, (u32)tb * 4, EP);
      }
    }
    {
      const int wd = wr & 1, wt2 = wr >> 1;
      const int tl = wt2 * 32 + (lane & 31);
      const u32 ha0 = (u32)(hh * 1024 + tl * 16);
      const u32 wa0 = (u32)((8 * hh + q4) * 128 + (((4 * wd + 2 * (p4 & 1) + blk) ^ ((q4 & 2) << 1)) << 4) + 8 * (p4 >> 1));
      const int dl = wd * 32 + 16 * hh;
      u32x4 xv0, xv1;
      {
        u32 go = (u32)((bm2 * 64 + tl) * 1024 + (bn2 * 64 + dl) * 2);
        asm volatile("global_load_dwordx4 %0, %2, %3\n\tglobal_load_dwordx4 %1, %2, %3 offset:16" : "=&v"(xv0), "=&v"(xv1) : "v"(go), "s"(x) : "memory");
      }
      f32x16 acc;
#pragma unroll
      for (int i = 0; i < 16; i++) acc[i] = 0.f;
      bf16x8 FH[2][8], FW[2][8];
      auto rdf = [&](int s, int ks) {
        FH[s & 1][ks] = ldsld<bf16x8>(hslot(s) + ha0 + ks * 2048);
        u32 a = wslot(s) + wa0 + ks * 2048;
        u32x2 a0 = ldstr(a), a1 = ldstr(a + 512);
        FW[s & 1][ks] = __builtin_bit_cast(bf16x8, (u32x4){a0[0], a0[1], a1[0], a1[1]});
      };
      bar();
      bar();
      STAMP(8);
#pragma unroll
      for (int ks = 0; ks < 8; ks++) rdf(0, ks);
#pragma unroll
      for (int s = 0; s < 14; s++) {
        if (s < 13) bar();
#pragma unroll
        for (int ks = 0; ks < 8; ks++) {
          if (s < 13) rdf(s + 1, ks);
          acc = __builtin_amdgcn_mfma_f32_32x32x16_bf16(FW[s & 1][ks], FH[s & 1][ks], acc, 0, 0, 0);
          __builtin_amdgcn_sched_barrier(0);
        }
      }
      STAMP(9);
      asm volatile("s_waitcnt vmcnt(0)" : "+v"(xv0), "+v"(xv1)::"memory");
      u32 ya = L_SPARE + (u32)((dl + q4) * 128 + (p4 >> 1) * 64 + (wt2 * 2 + blk) * 16 + (p4 & 1) * 8);
#pragma unroll
      for (int hf = 0; hf < 2; hf++) {
        u32x2 y0 = ldstr(ya + hf * 1024), y1 = ldstr(ya + hf * 1024 + 512);
        u32x4 yy = {y0[0], y0[1], y1[0], y1[1]};
        u32x4 xv = hf ? xv1 : xv0;
        u32x4 ov;
#pragma unroll
        for (int j = 0; j < 4; j++) {
          u32 s1 = pkbf(bf2f(xv[j]) + bf2f(yy[j]), bfhi(xv[j]) + bfhi(yy[j]));
          u32 s2 = pkbf(acc[8 * hf + 2 * j], acc[8 * hf + 2 * j + 1]);
          ov[j] = pkbf(bf2f(s1) + bf2f(s2), bfhi(s1) + bfhi(s2));
        }
        ldsst<u32x4>(L_SPARE + 8192 + (u32)(tl * 128 + (((4 * wd + 2 * hh + hf) ^ (tl & 7)) << 4)), ov);
      }
    }
  } else {
    const int bt = tid - 256;
    float kinv0, kinv1;
    u32 PK[2][5], QK[2][5];
    if (isg1) waitv<10>();
    bar();
    if (isg1) issue1(2);
    {
      u32 ad[11];
#pragma unroll
      for (int e = 0; e < 11; e++) {
        int lag = 2047 - 8 * bt - e;
        u32 u = __float_as_uint((float)lag);
        u32 id = ((u + 0x7fffu + ((u >> 16) & 1u)) >> 16) - 0x4300u + 128u;
        id = lag < 128 ? (u32)(lag < 0 ? 0 : lag) : id;
        ad[e] = L_KREG + 4096 + id * 4 + (id >= 384u ? 16384u : 0u);
      }
#pragma unroll
      for (int ch = 0; ch < 2; ch++) {
        float bnd = ldsld<float>(L_KREG + ch * 32768 + 4096 + 2816) + ldsld<float>(L_KREG + ch * 32768 + 8192 + 4096 + 2816);
        float ks, ki;
        pow2scale(bnd, 14, ks, ki);
        if (ch) kinv1 = ki; else kinv0 = ki;
        float v[11];
#pragma unroll
        for (int e = 0; e < 11; e++) {
          float t = ldsld<float>(ad[e] + ch * 32768) + ldsld<float>(ad[e] + ch * 32768 + 8192);
          v[e] = (e >= 8 && bt == 255) ? 0.f : t * ks;
        }
#pragma unroll
        for (int j = 0; j < 5; j++) { PK[ch][j] = pkh(v[2 * j], v[2 * j + 1]); QK[ch][j] = pkh(v[2 * j + 1], v[2 * j + 2]); }
      }
    }
    STAMP(11);
    if (isg1) waitv<10>();
    bar();
    STAMP(12);
    if (isg1) issue1(3);
#pragma unroll
    for (int ch = 0; ch < 2; ch++) {
      u32 a = L_KCP + ch * 16640 + bt * 16;
      ldsst<u32x4>(a, (u32x4){PK[ch][0], PK[ch][1], PK[ch][2], PK[ch][3]});
      ldsst<u32x4>(a + 4160, (u32x4){QK[ch][0], QK[ch][1], QK[ch][2], QK[ch][3]});
      ldsst<u32x4>(a + 8320, (u32x4){PK[ch][1], PK[ch][2], PK[ch][3], PK[ch][4]});
      ldsst<u32x4>(a + 12480, (u32x4){QK[ch][1], QK[ch][2], QK[ch][3], QK[ch][4]});
    }
    if (bt < 16) ldsst<u32x4>(L_KCP + (bt >> 1) * 4160 + (256 + (bt & 1)) * 16, (u32x4){0, 0, 0, 0});
    if (lane < 32) ldsst<u32x4>(L_UL + wr * 4608 + lane * 16, (u32x4){0, 0, 0, 0});
    if (isg1) waitv<8>(); else waitv<0>();
    STAMP(13);
    bar();
    STAMP(14);
#pragma unroll
    for (int s = 3; s <= 5; s++) {
      if (isg1) { issue1(s + 1); waitv<8>(); }
      bar();
    }
    if (isg1) issue1(7);
    u32x2 gv;
    u32x4 uf;
    u32 bv;
    const void* gbase = wr < 2 ? (const void*)(ws + WS_STAT + (size_t)bm * 1024) : (const void*)(ws + WS_CS + (size_t)bn * 1024 - 1024);
    {
      int bidx = (isg1 ? bn * 128 : 0) + (bt & 127);
      asm volatile("global_load_dwordx2 %0, %3, %6 sc1\n\tglobal_load_dwordx4 %1, %4, %7 sc1\n\tglobal_load_ushort %2, %5, %8"
                   : "=&v"(gv), "=&v"(uf), "=&v"(bv)
                   : "v"((u32)bt * 8), "v"((u32)lane * 16), "v"((u32)bidx * 2), "s"(isg1 ? gbase : (const void*)ws), "s"(ws + WS_UFLAG), "s"(b1)
                   : "memory");
    }
    STAMP(15);
    waitv<11>();
    bar();
    STAMP(16);
    waitv<3>();
    bar();
    STAMP(17);
    asm volatile("s_waitcnt vmcnt(0)" : "+v"(gv), "+v"(uf), "+v"(bv)::"memory");
    if (isg1) {
      u32 spins = 0;
      while (!__all(gv[1] == EP) && ++spins < SPIN_MAX) gv = ld8c_wait(gbase, (u32)bt * 8);
      if (wr < 2) {
        float m = bf2f(gv[0]), sd = bfhi(gv[0]);
        float r = 1.f / sd;
        ldsst<f32x2>(L_EV + 1024 + bt * 8, (f32x2){r, r * m});
      } else {
        int f = bt - 128;
        ldsst<float>(L_EV + (f >> 1) * 16 + (f & 1) * 4, __uint_as_float(gv[0]));
        ldsst<float>(L_EV + (f >> 1) * 16 + (f & 1) * 4 + 8, bf2f(bv));
      }
    }
    STAMP(18);
    bar();
    STAMP(4);
    {
      u32 spins = 0;
      while (!__all(uf[0] == EP && uf[1] == EP && uf[2] == EP && uf[3] == EP) && ++spins < SPIN_MAX)
        uf = ld16c_wait(ws + WS_UFLAG, (u32)lane * 16);
    }
    const int ch = wr >> 1, cset = wr & 1;
    {
      u32 vo = (u32)(lane * 8192 + ch * 16);
      glds4<1024, true>(ws + WS_U + (size_t)tb * 32, vo, vo + 524288, vo + 1048576, vo + 1572864, L_UL + wr * 4608 + 512);
    }
    u32 voW2;
    {
      int row = 32 * wr + (lane >> 3);
      voW2 = (u32)(row * 1024 + (((lane & 7) ^ ((row & 2) << 1)) << 4));
    }
    const char* w2b = (const char*)w2 + bn2 * 128;
    auto issue2w = [&](int s, u32 slot) {
      int kt = s + rot2; kt = kt >= 14 ? kt - 14 : kt;
      glds4<1024, false>(w2b + (size_t)kt * 131072, voW2, voW2 + 8192, voW2 + 16384, voW2 + 24576, slot + wr * 4096);
    };
    issue2w(0, wslot(0));
    u32 dskv = Dsk[2 * tb + ch];
    waitv<4>();
    STAMP(5);
    {
      const int n = lane & 15, kg = lane >> 4;
      f32x4 acc[4];
#pragma unroll
      for (int a = 0; a < 4; a++) acc[a] = (f32x4){0.f, 0.f, 0.f, 0.f};
      const int cc = (3 - n) & 3;
      u32 ub = L_UL + wr * 4608 + (u32)((256 + 16 * n - 16 + 8 * kg) * 2);
      u32 ab = L_KCP + ch * 16640 + cc * 4160 + (u32)((2031 - n + 8 * kg - cc - 1792) * 2);
#pragma unroll 1
      for (int it = 0; it < 8; it++) {
        if (it >= 1 && it <= 4) issue2w(it, (u32)it * 16384u);
        if (cset) conv_body<1>(acc, ub, ab); else conv_body<0>(acc, ub, ab);
        ub -= 64; ab -= 64;
      }
      STAMP(6);
      const float dsk = bf2f(dskv), kinv = ch ? kinv1 : kinv0;
      char* yp = ws + WS_Y + (size_t)(2 * tb + ch) * 4096;
#pragma unroll
      for (int pr = 0; pr < 2; pr++) {
        u32 pk[2][2];
#pragma unroll
        for (int j = 0; j < 2; j++) {
          int a = cset ? (pr ? 5 + j : 1 + j) : (pr ? 4 + 3 * j : 3 * j);
          int t = 256 * a + 16 * n + 4 * kg;
          f16x4 uu = ldsld<f16x4>(L_UL + wr * 4608 + 512 + t * 2);
          f32x4 yy = acc[2 * pr + j];
          u32 y01 = pkbf(yy[0] * kinv, yy[1] * kinv), y23 = pkbf(yy[2] * kinv, yy[3] * kinv);
          u32 s01 = pkbf((float)uu[0] * dsk, (float)uu[1] * dsk), s23 = pkbf((float)uu[2] * dsk, (float)uu[3] * dsk);
          pk[j][0] = pkbf(bf2f(y01) + bf2f(s01), bfhi(y01) + bfhi(s01));
          pk[j][1] = pkbf(bf2f(y23) + bf2f(s23), bfhi(y23) + bfhi(s23));
        }
        auto s0 = __builtin_amdgcn_permlane16_swap(pk[0][0], pk[1][0], false, false);
        auto s1 = __builtin_amdgcn_permlane16_swap(pk[0][1], pk[1][1], false, false);
        int a0 = cset ? (pr ? 5 : 1) : (pr ? 4 : 0), a1 = cset ? (pr ? 6 : 2) : (pr ? 7 : 3);
        int as = (kg & 1) ? a1 : a0;
        st16c(yp, (u32)(as * 512 + (kg >> 1) * 256 + n * 16), (u32x4){s0[0], s1[0], s0[1], s1[1]});
      }
    }
    waitv<0>();
    if (lane == 0) st4c(ws + WS_YFLAG, (u32)(cset * 512 + 2 * tb + ch) * 4, EP);
    STAMP(7);
    {
      u32 spins = 0;
      for (;;) {
        u32 f = ld4c_wait(ws + WS_HFLAG + (size_t)bm2 * 128, (u32)(lane < 28 ? lane : 0) * 4);
        if (__all(f == EP) || ++spins > SPIN_MAX) break;
      }
    }
    STAMP(8);
    const char* hb = ws + WS_H + (size_t)(bm2 >> 1) * 224 * 2048 + (bm2 & 1) * 1024 + wr * 8192;
    const u32 voH = (u32)lane * 16;
    auto issue2h = [&](int s) {
      int kt = s + rot2; kt = kt >= 14 ? kt - 14 : kt;
      glds4<1024, true>(hb + (size_t)kt * 32768, voH, voH + 2048, voH + 4096, voH + 6144, hslot(s) + wr * 4096);
    };
    bar();
    issue2h(0); issue2h(1); issue2h(2); issue2h(3);
    u32 yf;
    const int ya_ = bm2 >> 2;
    const int yset = (ya_ == 1 || ya_ == 2 || ya_ == 5 || ya_ == 6) ? 1 : 0;
    const void* yfb = ws + WS_YFLAG + (size_t)(yset * 512 + bn2 * 64) * 4;
    auto step = [&](auto S_) {
      constexpr int j = decltype(S_)::value;
      waitv<g2_wait(j)>();
      bar();
      if constexpr (j >= 1) {
        if constexpr (j + 3 <= 13) issue2h(j + 3);
        if constexpr (j + 4 <= 13) issue2w(j + 4, wslot(j + 4));
        if constexpr (j == 5)
          asm volatile("global_load_dword %0, %1, %2 sc1" : "=&v"(yf) : "v"((u32)lane * 4), "s"(yfb) : "memory");
        if constexpr (j == 9) {
          asm volatile("" : "+v"(yf)::"memory");
          u32 spins = 0;
          while (!__all(yf == EP) && ++spins < SPIN_MAX) yf = ld4c_wait(yfb, (u32)lane * 4);
          const char* yb = ws + WS_Y + (size_t)bn2 * 64 * 4096 + ya_ * 512 + (bm2 & 3) * 64;
          u32 vo = (u32)((16 * wr + (lane >> 3)) * 4096 + ((lane >> 2) & 1) * 256 + (lane & 3) * 16);
          glds1c(yb, vo, L_SPARE + wr * 2048);
          glds1c(yb, vo + 32768, L_SPARE + wr * 2048 + 1024);
        }
      }
    };
#define G2S(n) step(std::integral_constant<int, n>{});
    G2S(0) G2S(1) G2S(2) G2S(3) G2S(4) G2S(5) G2S(6) G2S(7) G2S(8) G2S(9) G2S(10) G2S(11) G2S(12) G2S(13)
    STAMP(9);
  }
  bar();
  {
    int t = tid >> 3, c = tid & 7;
    u32x4 ov = ldsld<u32x4>(L_SPARE + 8192 + (u32)(t * 128 + ((c ^ (t & 7)) << 4)));
    st16((char*)out + (size_t)bm2 * 65536 + bn2 * 128, (u32)(t * 1024 + c * 16), ov);
  }
  STAMP(10);
}

extern "C" void kernel_launch(void* const* d_in, const int* in_sizes, int n_in, void* d_out, int out_size, void* d_ws,
                              size_t ws_size, hipStream_t stream) {
  (void)hipMemsetAsync(d_ws, 0, WS_SYNC_BYTES, stream);
  fused_kernel<<<256, 512, 0, stream>>>((const u16*)d_in[0], (const u16*)d_in[1], (const u16*)d_in[2], (const u16*)d_in[3],
                                        (const u16*)d_in[4], (const u16*)d_in[5], (const u16*)d_in[6], (const u16*)d_in[7],
                                        (const u16*)d_in[8], (const u16*)d_in[10], (const u16*)d_in[11], (const u16*)d_in[12],
                                        (u16*)d_out, (char*)d_ws);
}
```

```cpp
#include <hip/hip_runtime.h>
#include <stdint.h>
#include <type_traits>

typedef unsigned short u16;
typedef unsigned int u32;
typedef unsigned long long u64;
typedef __attribute__((ext_vector_type(8))) __bf16 bf16x8;
typedef __attribute__((ext_vector_type(2))) __bf16 bf16x2;
typedef __attribute__((ext_vector_type(8))) _Float16 f16x8;
typedef __attribute__((ext_vector_type(4))) _Float16 f16x4;
typedef __attribute__((ext_vector_type(2))) _Float16 f16x2;
typedef __attribute__((ext_vector_type(2))) float f32x2;
typedef __attribute__((ext_vector_type(4))) float f32x4;
typedef __attribute__((ext_vector_type(16))) float f32x16;
typedef __attribute__((ext_vector_type(4))) u32 u32x4;
typedef __attribute__((ext_vector_type(2))) u32 u32x2;
typedef __attribute__((ext_vector_type(4))) short s16x4;
#define AS3 __attribute__((address_space(3)))
#define DEVI __device__ __forceinline__

constexpr size_t WS_STAT = 0, WS_CS = 16384, WS_UFLAG = 30720, WS_YFLAG = 31744  , WS_HFLAG = 35840  ,
                 WS_SYNC_BYTES = 39936, WS_R = 40960  , WS_A = 43008  , WS_N = 44032;
constexpr size_t WS_U = 1u << 20, WS_Y = 4u << 20, WS_H = 8u << 20, WS_DBG = 32u << 20;
constexpr u32 L_LNS = 65536;
constexpr u32 L_KREG = 98304;
constexpr u32 L_UL = L_KREG;
constexpr u32 L_KCP = L_KREG + 18432;
constexpr u32 L_EV = L_KCP + 33280;
constexpr u32 L_SPARE = L_KREG + 49152;
constexpr u32 SPIN_MAX = 1u << 15;

template <typename T> DEVI T ldsld(u32 a) { return *(AS3 T*)(uintptr_t)a; }
template <typename T> DEVI void ldsst(u32 a, T v) { *(AS3 T*)(uintptr_t)a = v; }
DEVI u32x2 ldstr(u32 a) { return __builtin_bit_cast(u32x2, __builtin_amdgcn_ds_read_tr16_b64_v4i16((AS3 s16x4*)(uintptr_t)a)); }
DEVI float bf2f(u32 v) { return __uint_as_float(v << 16); }
DEVI float bfhi(u32 v) { return __uint_as_float(v & 0xffff0000u); }
DEVI float rbf(float f) { return (float)(__bf16)f; }
DEVI u32 pkbf(float a, float b) { bf16x2 v = {(__bf16)a, (__bf16)b}; return __builtin_bit_cast(u32, v); }
DEVI u32 pkh(float a, float b) { f16x2 v = {(_Float16)a, (_Float16)b}; return __builtin_bit_cast(u32, v); }

template <int STEP, bool COH>
DEVI void glds4(const void* sbase, u32 v0, u32 v1, u32 v2, u32 v3, u32 lds0) {
  u32 keep;
  if (COH)
    asm volatile(
        "s_mov_b32 %0, m0\n\ts_mov_b32 m0, %6\n\ts_nop 0\n\tglobal_load_lds_dwordx4 %1, %5 sc1\n\t"
        "s_add_u32 m0, m0, %7\n\ts_nop 0\n\tglobal_load_lds_dwordx4 %2, %5 sc1\n\t"
        "s_add_u32 m0, m0, %7\n\ts_nop 0\n\tglobal_load_lds_dwordx4 %3, %5 sc1\n\t"
        "s_add_u32 m0, m0, %7\n\ts_nop 0\n\tglobal_load_lds_dwordx4 %4, %5 sc1\n\ts_mov_b32 m0, %0"
        : "=&s"(keep) : "v"(v0), "v"(v1), "v"(v2), "v"(v3), "s"(sbase), "s"(lds0), "n"(STEP) : "memory", "scc");
  else
    asm volatile(
        "s_mov_b32 %0, m0\n\ts_mov_b32 m0, %6\n\ts_nop 0\n\tglobal_load_lds_dwordx4 %1, %5\n\t"
        "s_add_u32 m0, m0, %7\n\ts_nop 0\n\tglobal_load_lds_dwordx4 %2, %5\n\t"
        "s_add_u32 m0, m0, %7\n\ts_nop 0\n\tglobal_load_lds_dwordx4 %3, %5\n\t"
        "s_add_u32 m0, m0, %7\n\ts_nop 0\n\tglobal_load_lds_dwordx4 %4, %5\n\ts_mov_b32 m0, %0"
        : "=&s"(keep) : "v"(v0), "v"(v1), "v"(v2), "v"(v3), "s"(sbase), "s"(lds0), "n"(STEP) : "memory", "scc");
}
DEVI void glds1c(const void* sbase, u32 voff, u32 ldsaddr) {
  u32 keep;
  asm volatile("s_mov_b32 %0, m0\n\ts_mov_b32 m0, %3\n\ts_nop 0\n\tglobal_load_lds_dwordx4 %1, %2 sc1\n\ts_mov_b32 m0, %0"
               : "=&s"(keep) : "v"(voff), "s"(sbase), "s"(ldsaddr) : "memory");
}
DEVI void st16c(void* sbase, u32 voff, u32x4 v) {
  asm volatile("global_store_dwordx4 %0, %1, %2 sc1\n\ts_nop 1" ::"v"(voff), "v"(v), "s"(sbase) : "memory");
}
DEVI void st16(void* sbase, u32 voff, u32x4 v) {
  asm volatile("global_store_dwordx4 %0, %1, %2\n\ts_nop 1" ::"v"(voff), "v"(v), "s"(sbase) : "memory");
}
DEVI void st8c(void* sbase, u32 voff, u32x2 v) {
  asm volatile("global_store_dwordx2 %0, %1, %2 sc1\n\ts_nop 1" ::"v"(voff), "v"(v), "s"(sbase) : "memory");
}
DEVI void st4c(void* sbase, u32 voff, u32 v) {
  asm volatile("global_store_dword %0, %1, %2 sc1\n\ts_nop 1" ::"v"(voff), "v"(v), "s"(sbase) : "memory");
}
DEVI u32x2 ld8c_wait(const void* sbase, u32 voff) {
  u32x2 r;
  asm volatile("global_load_dwordx2 %0, %1, %2 sc1\n\ts_waitcnt vmcnt(0)" : "=&v"(r) : "v"(voff), "s"(sbase) : "memory");
  return r;
}
DEVI u32 ld4c_wait(const void* sbase, u32 voff) {
  u32 r;
  asm volatile("global_load_dword %0, %1, %2 sc1\n\ts_waitcnt vmcnt(0)" : "=&v"(r) : "v"(voff), "s"(sbase) : "memory");
  return r;
}
DEVI u32x4 ld16c_wait(const void* sbase, u32 voff) {
  u32x4 r;
  asm volatile("global_load_dwordx4 %0, %1, %2 sc1\n\ts_waitcnt vmcnt(0)" : "=&v"(r) : "v"(voff), "s"(sbase) : "memory");
  return r;
}
template <int N> DEVI void waitv() { asm volatile("s_waitcnt vmcnt(%0)" ::"n"(N) : "memory"); }
DEVI void bar() {
  asm volatile("s_waitcnt lgkmcnt(0)" ::: "memory");
  __builtin_amdgcn_s_barrier();
  asm volatile("" ::: "memory");
}
template <int CTRL, int RM> DEVI float dppf(float old, float v) {
  return __int_as_float(__builtin_amdgcn_update_dpp(__float_as_int(old), __float_as_int(v), CTRL, RM, 0xf, false));
}
DEVI float wave_sum(float v) {
  v += dppf<0x111, 0xf>(0.f, v); v += dppf<0x112, 0xf>(0.f, v); v += dppf<0x114, 0xf>(0.f, v); v += dppf<0x118, 0xf>(0.f, v);
  v += dppf<0x142, 0xa>(0.f, v); v += dppf<0x143, 0xc>(0.f, v);
  return __int_as_float(__builtin_amdgcn_readlane(__float_as_int(v), 63));
}
DEVI float wave_max0(float v) {
  v = fmaxf(v, dppf<0x111, 0xf>(0.f, v)); v = fmaxf(v, dppf<0x112, 0xf>(0.f, v)); v = fmaxf(v, dppf<0x114, 0xf>(0.f, v));
  v = fmaxf(v, dppf<0x118, 0xf>(0.f, v)); v = fmaxf(v, dppf<0x142, 0xa>(0.f, v)); v = fmaxf(v, dppf<0x143, 0xc>(0.f, v));
  return __int_as_float(__builtin_amdgcn_readlane(__float_as_int(v), 63));
}
DEVI f32x2 cmul(f32x2 a, f32x2 b) {
  f32x2 t, r;
  asm("v_pk_mul_f32 %0, %1, %2 op_sel_hi:[0,1]" : "=v"(t) : "v"(a), "v"(b));
  asm("v_pk_fma_f32 %0, %1, %2, %3 op_sel:[1,1,0] op_sel_hi:[1,0,1] neg_lo:[1,0,0]" : "=v"(r) : "v"(a), "v"(b), "v"(t));
  return r;
}
DEVI f32x2 cpow(float a2, float fhi, float flo, float R) {
  float m = __builtin_amdgcn_exp2f(a2 * R);
  float ph = __builtin_fmaf(flo, R, __builtin_amdgcn_fractf(fhi * R));
  return (f32x2){m * __builtin_amdgcn_cosf(ph), m * __builtin_amdgcn_sinf(ph)};
}
DEVI u32 kidx_exact(int l) {
  u32 u = __float_as_uint((float)l) >> 16;
  return l < 128 ? (u32)l : u - 0x4300u + 128u;
}
DEVI void pow2scale(float mx, int target, float& s, float& inv) {
  int ex = (int)((__float_as_uint(mx) >> 23) & 0xff);
  int se = 254 + target - ex;
  se = se < 2 ? 2 : (se > 252 ? 252 : se);
  s = __uint_as_float((u32)se << 23);
  inv = __uint_as_float((u32)(254 - se) << 23);
}
constexpr int g2_ops(int j) { return (j + 3 <= 13 ? 4 : 0) + (j + 4 <= 13 ? 4 : 0) + (j == 5 ? 1 : 0) + (j == 9 ? 2 : 0); }
constexpr int g2_wait(int s) {
  if (s < 4) { int n = (3 - s) * 4; for (int j = 1; j <= s - 1; j++) n += g2_ops(j); return n; }
  return (g2_ops(s - 3) - 4) + g2_ops(s - 2) + g2_ops(s - 1);
}
constexpr u32 hslot(int s) { return (s & 3) == 0 ? 81920u : L_KREG + (u32)((s & 3) - 1) * 16384u; }
constexpr u32 wslot(int s) { return (u32)(s % 5) * 16384u; }

template <int SET>
DEVI void conv_body(f32x4 (&acc)[4], u32 ub, u32 ab) {
  constexpr int A0 = SET ? 1 : 0, A1 = SET ? 2 : 3, A2 = SET ? 5 : 4, A3 = SET ? 6 : 7;
  f16x8 Bq[8];
#pragma unroll
  for (int q = 0; q <= A3; q++) Bq[q] = ldsld<f16x8>(ub + q * 512);
#pragma unroll
  for (int i = 0; i <= A3; i++) {
    u32x2 a0 = *(volatile AS3 u32x2*)(uintptr_t)(ab + (7 - i) * 512);
    u32x2 a1 = *(volatile AS3 u32x2*)(uintptr_t)(ab + (7 - i) * 512 + 8);
    f16x8 A = __builtin_bit_cast(f16x8, (u32x4){a0[0], a0[1], a1[0], a1[1]});
    if (A3 >= i) acc[3] = __builtin_amdgcn_mfma_f32_16x16x32_f16(A, Bq[A3 - i], acc[3], 0, 0, 0);
    if (A2 >= i) acc[2] = __builtin_amdgcn_mfma_f32_16x16x32_f16(A, Bq[A2 >= i ? A2 - i : 0], acc[2], 0, 0, 0);
    if (A1 >= i) acc[1] = __builtin_amdgcn_mfma_f32_16x16x32_f16(A, Bq[A1 >= i ? A1 - i : 0], acc[1], 0, 0, 0);
    if (A0 >= i) acc[0] = __builtin_amdgcn_mfma_f32_16x16x32_f16(A, Bq[A0 >= i ? A0 - i : 0], acc[0], 0, 0, 0);
  }
}

__global__ __launch_bounds__(512, 2) void fused_kernel(
    const u16* __restrict__ x, const u16* __restrict__ Are, const u16* __restrict__ Aim, const u16* __restrict__ Bre,
    const u16* __restrict__ Bim, const u16* __restrict__ Cre, const u16* __restrict__ Cim, const u16* __restrict__ ldt,
    const u16* __restrict__ Dsk, const u16* __restrict__ w1, const u16* __restrict__ b1, const u16* __restrict__ w2,
    u16* __restrict__ out, char* __restrict__ ws) {
  __shared__ __attribute__((aligned(16))) char sm[163840];
  asm volatile("" ::"v"(sm) : "memory");
  const u32 EP = 1u;
  const int tid = threadIdx.x, lane = tid & 63;
  const int w = __builtin_amdgcn_readfirstlane(tid >> 6);
  const int b = blockIdx.x, xcd = b & 7, bi = b >> 3;
  const bool isg1 = bi < 28;
  const int bm = 2 * xcd + (bi & 1), bn = bi >> 1;
  const int tb = 32 * xcd + bi;
  const int rot1 = (2 * bm + bn) & 7;
  const int bm2 = 4 * xcd + (bi & 3), bn2 = bi >> 2;
  const int rot2 = (bi * 5) % 14;
  const int wr = w & 3;
#define STAMP(i)
  STAMP(0);
  const int kch = w >> 2, kgrp = (w >> 1) & 1, kmh = w & 1;
  const int dch = 2 * tb + kch;
  u32 leadN = 0;
  u32x4 xr;
  u32 pa, pb, pc_, pd, pe, pf, pl;
  {
    u32 vo = (u32)((8 * tb + w) * 1024 + lane * 16);
    u32 po = (u32)((dch * 128 + kmh * 64 + lane) * 2);
    u32 lo = (u32)(dch * 2);
    asm volatile(
        "global_load_dwordx4 %0, %8, %11\n\t"
        "global_load_ushort %1, %9, %12\n\t"
        "global_load_ushort %2, %9, %13\n\t"
        "global_load_ushort %3, %9, %14\n\t"
        "global_load_ushort %4, %9, %15\n\t"
        "global_load_ushort %5, %9, %16\n\t"
        "global_load_ushort %6, %9, %17\n\t"
        "global_load_ushort %7, %10, %18"
        : "=&v"(xr), "=&v"(pa), "=&v"(pb), "=&v"(pc_), "=&v"(pd), "=&v"(pe), "=&v"(pf), "=&v"(pl)
        : "v"(vo), "v"(po), "v"(lo), "s"(x), "s"(Are), "s"(Aim), "s"(Bre), "s"(Bim), "s"(Cre), "s"(Cim), "s"(ldt)
        : "memory");
  }
  u32 a_old;
  asm volatile("global_load_dword %0, %1, %2 sc1" : "=&v"(a_old) : "v"((u32)b * 4), "s"(ws + WS_A) : "memory");
  {
    u32 off = 0; bool act = false;
    if (lane == 0) { off = (u32)(WS_STAT + (8 * tb + w) * 8 + 4); act = true; }
    if (w == 0 && lane == 1) { off = (u32)(WS_UFLAG + tb * 4); act = true; }
    if (w >= 4 && lane == 1) { off = (u32)(WS_YFLAG + ((w & 1) * 512 + 2 * tb + ((w >> 1) & 1)) * 4); act = true; }
    if (w < 4 && lane == 2 && isg1) { off = (u32)(WS_HFLAG + ((bm * 2 + (w >> 1)) * 32 + bn * 2 + (w & 1)) * 4); act = true; }
    if (act) st4c(ws, off, 0u);
    if (!isg1 && w == 0 && (xcd * 4 + bi - 28) < 28) st4c(ws + WS_CS + (size_t)(xcd * 4 + bi - 28) * 512 + 4, (u32)lane * 8, 0u);
  }
  if (b == 255 && w == 1) {
    u32x4 r0 = ld16c_wait(ws + WS_R, (u32)lane * 32), r1 = ld16c_wait(ws + WS_R, (u32)lane * 32 + 16);
    u32 nold = ld4c_wait(ws + WS_N, 0);
    u32 cand = nold + 1;
    while (__any(r0[1] == cand || r0[3] == cand || r1[1] == cand || r1[3] == cand)) cand++;
    if (cand == nold) cand++;
    if (lane == 0) st4c(ws + WS_N, 0, cand);
    leadN = cand;
  }
  waitv<0>();
  __builtin_amdgcn_s_barrier();
  u32 voX, voW;
  {
    int row = 32 * wr + (lane >> 3);
    voX = (u32)(row * 1024 + (((lane & 7) ^ ((row >> 1) & 7)) << 4));
    int k = 16 * wr + (lane >> 4);
    voW = (u32)(k * 3584 + (((lane & 15) ^ (((k & 1) << 1) | ((k & 2) << 2))) << 4));
  }
  const char* xb = (const char*)x + (size_t)bm * 131072;
  const char* wb = (const char*)w1 + bn * 256;
  auto issue1 = [&](int s) {
    int kt = (s + rot1) & 7;
    u32 st = (u32)((s % 3) * 32768 + wr * 4096);
    glds4<1024, false>(xb + kt * 128, voX, (voX ^ 64u) + 8192, voX + 16384, (voX ^ 64u) + 24576, st);
    glds4<1024, false>(wb + (size_t)kt * 229376, voW, voW + 14336, voW + 28672, voW + 43008, st + 16384);
  };
  const int ci = xcd * 4 + (bi - 28);
  if (w >= 4) {
    if (isg1) { issue1(0); issue1(1); waitv<16>(); }
    else waitv<0>();
  } else {
    if (isg1) waitv<0>();
    else {
      int cc = ci < 28 ? ci : 27;
      u32 vo = (u32)((8 * wr + (lane >> 3)) * 3584 + (lane & 7) * 16);
      const char* cb = (const char*)w1 + cc * 128;
#pragma unroll
      for (int j = 0; j < 4; j++)
        glds4<4096, false>(cb + (size_t)j * 128 * 3584, vo, vo + 32 * 3584, vo + 64 * 3584, vo + 96 * 3584, (u32)(j * 16384 + wr * 1024));
      waitv<16>();
    }
  }
  asm volatile("" : "+v"(a_old)::"memory");
  asm volatile("" : "+v"(xr), "+v"(pa), "+v"(pb), "+v"(pc_), "+v"(pd), "+v"(pe), "+v"(pf), "+v"(pl)::"memory");
  STAMP(1);
  {
    float v[8];
#pragma unroll
    for (int j = 0; j < 4; j++) { v[2 * j] = bf2f(xr[j]); v[2 * j + 1] = bfhi(xr[j]); }
    float s = ((v[0] + v[1]) + (v[2] + v[3])) + ((v[4] + v[5]) + (v[6] + v[7]));
    float mean = wave_sum(s) * (1.f / 512.f);
    float q = 0.f;
#pragma unroll
    for (int j = 0; j < 8; j++) { float d = v[j] - mean; q = __builtin_fmaf(d, d, q); }
    float var = wave_sum(q) * (1.f / 512.f);
    float mb = rbf(mean), sd = rbf(__builtin_sqrtf(rbf(rbf(var) + 1e-5f)));
    float rs = 1.f / sd;
    u32x4 o;
#pragma unroll
    for (int j = 0; j < 4; j++) {
      u32 dd = pkbf(v[2 * j] - mb, v[2 * j + 1] - mb);
      float d0 = bf2f(dd), d1 = bfhi(dd);
      float q0 = d0 * rs, q1 = d1 * rs;
      q0 = __builtin_fmaf(__builtin_fmaf(-q0, sd, d0), rs, q0);
      q1 = __builtin_fmaf(__builtin_fmaf(-q1, sd, d1), rs, q1);
      u32 qq = pkbf(q0, q1);
      o[j] = pkh(bf2f(qq), bfhi(qq));
    }
    ldsst<u32x4>(L_LNS + w * 1088 + lane * 16, o);
    bar();
    int li = lane & 15;
    u32 a = L_LNS + (li >> 2) * 1088 + ((tid & ~15) + 4 * (li & 3)) * 2;
    u32x2 p0 = ldstr(a), p1 = ldstr(a + 4 * 1088);
    st16c(ws + WS_U + (size_t)tb * 8192, (u32)tid * 16, (u32x4){p0[0], p0[1], p1[0], p1[1]});
    u32 pay = (__float_as_uint(mb) >> 16) | (__float_as_uint(sd) & 0xffff0000u);
    if (lane == 0) st8c(ws + WS_STAT + (size_t)(8 * tb + w) * 8, 0, (u32x2){pay, EP});
  }
  STAMP(2);
  {
    float dt = rbf(expf(bf2f(pl)));
    float a2 = (dt * bf2f(pa)) * 1.44269504088896341f;
    float bb = dt * bf2f(pb);
    f32x2 W = cmul((f32x2){bf2f(pe), bf2f(pf)}, (f32x2){dt * bf2f(pc_), dt * bf2f(pd)});
    const float C_HI = 0.15915494309189535f, C_LO = (float)(0.15915494309189533577 - (double)0.15915494309189535f);
    float p = bb * C_HI;
    float er = __builtin_fmaf(bb, C_HI, -p);
    float fhi = __uint_as_float(__float_as_uint(p) & 0xfffff000u);
    float flo = (p - fhi) + __builtin_fmaf(bb, C_LO, er);
    float g = 0.f;
    if (__any(a2 > 0.f)) { g = wave_max0(fmaxf(a2, 0.f)); a2 -= g; }
    float wsum = wave_sum(fabsf(W[0]) + fabsf(W[1]));
    float sig, isig;
    pow2scale(wsum, 13, sig, isig);
    const float cs = kgrp ? 8.f : 2.f, rs = kgrp ? 128.f : 32.f, rb = kgrp ? 512.f : 0.f;
    f32x2 zc = cpow(a2, fhi, flo, cs), zc4 = cpow(a2, fhi, flo, 4.f * cs);
    f32x2 zr = cpow(a2, fhi, flo, rs), zr4 = cpow(a2, fhi, flo, 4.f * rs);
    f32x2 zb = cpow(a2, fhi, flo, rb), zx = cpow(a2, fhi, flo, 0.5f * cs);
    zc[1] = -zc[1]; zc4[1] = -zc4[1];
    const u32 T = L_KREG + w * 8192;
    const u32 wbase = T + (lane >> 2) * 16 + (lane & 3) * 4;
    f32x2 c[4], r[8], rr4[4];
    c[0] = (f32x2){W[0] * sig, -W[1] * sig};
    c[1] = cmul(c[0], zc); c[2] = cmul(c[1], zc); c[3] = cmul(c[2], zc);
    rr4[0] = zb; rr4[1] = cmul(rr4[0], zr); rr4[2] = cmul(rr4[1], zr); rr4[3] = cmul(rr4[2], zr);
#pragma unroll
    for (int o = 0; o < 4; o++) {
#pragma unroll
      for (int j = 0; j < 4; j++) {
        int e = 4 * o + j;
        if (o > 0) { c[j] = cmul(c[j], zc4); rr4[j] = cmul(rr4[j], zr4); }
        if (o < 2) r[e] = rr4[j];
        u32 a = wbase ^ (u32)(e << 4);
        ldsst<u32>(a + e * 256, pkh(rr4[j][0], rr4[j][1]));
        ldsst<u32>(a + e * 256 + 4096, pkh(c[j][0], c[j][1]));
      }
    }
    const int rr = lane & 15, kg = lane >> 4;
    const u32 ra = T + rr * 256 + ((kg ^ rr) << 4);
    f16x8 bf[4];
    f32x4 d0 = {0.f, 0.f, 0.f, 0.f}, d1 = {0.f, 0.f, 0.f, 0.f};
#pragma unroll
    for (int s = 0; s < 4; s++) {
      f16x8 af = ldsld<f16x8>(ra ^ (u32)(s << 6));
      bf[s] = ldsld<f16x8>((ra ^ (u32)(s << 6)) + 4096);
      d0 = __builtin_amdgcn_mfma_f32_16x16x32_f16(af, bf[s], d0, 0, 0, 0);
    }
#pragma unroll
    for (int e = 0; e < 8; e++) {
      f32x2 t = cmul(r[e], zx);
      ldsst<u32>((wbase ^ (u32)(e << 4)) + e * 256, pkh(t[0], t[1]));
    }
#pragma unroll
    for (int s = 0; s < 4; s++) {
      f16x8 af = ldsld<f16x8>(ra ^ (u32)(s << 6));
      d1 = __builtin_amdgcn_mfma_f32_16x16x32_f16(af, bf[s], d1, 0, 0, 0);
    }
    const int sh = kgrp ? 2 : 0;
    const u32 P = T + 4096;
#pragma unroll
    for (int e = 0; e < 4; e++) {
      int l0 = (kgrp ? 512 : 0) + ((32 * (4 * kg + e) + 2 * rr) << sh);
      int l1 = l0 + (1 << sh);
      float s0 = isig, s1 = isig;
      if (g > 0.f) { s0 *= __builtin_amdgcn_exp2f(g * (float)l0); s1 *= __builtin_amdgcn_exp2f(g * (float)l1); }
      u32 i0 = l0 <= 2048 ? kidx_exact(l0) : 700u;
      u32 i1 = l1 < (256 << sh) ? kidx_exact(l1) : 701u;
      ldsst<float>(P + i0 * 4, d0[e] * s0);
      ldsst<float>(P + i1 * 4, d1[e] * s1);
    }
    if (lane == 0) ldsst<float>(P + 704 * 4, wsum * __builtin_amdgcn_exp2f(g * 2048.f));
  }
  STAMP(3);
  const int hh = lane >> 5, blk = (lane >> 4) & 1, q4 = (lane & 15) >> 2, p4 = lane & 3;
  if (w < 4) {
    __builtin_amdgcn_s_setprio(2);
    if (isg1) {
      const int wf = wr & 1, wt = wr >> 1;
      const int tl = wt * 64 + (lane & 31);
      const u32 xa0 = (u32)(tl * 128 + ((hh ^ ((tl >> 1) & 7)) << 4));
      const u32 wa0 = (u32)(16384 + (8 * hh + q4) * 256 +
                            (((8 * wf + 4 * (p4 & 1) + blk) ^ (((q4 & 1) << 1) | ((q4 & 2) << 2))) << 4) + 8 * (p4 >> 1));
      f32x16 acc[2][2];
#pragma unroll
      for (int i = 0; i < 16; i++) { acc[0][0][i] = 0.f; acc[0][1][i] = 0.f; acc[1][0][i] = 0.f; acc[1][1][i] = 0.f; }
      bf16x8 FX[2][4][2], FW[2][4][2];
      auto rdf = [&](int s, int ks) {
        const u32 sb = (u32)((s % 3) * 32768);
#pragma unroll
        for (int j = 0; j < 2; j++) FX[s & 1][ks][j] = ldsld<bf16x8>(sb + (xa0 ^ (u32)(ks << 5)) + j * 4096);
#pragma unroll
        for (int tau = 0; tau < 2; tau++) {
          u32 a = sb + (wa0 ^ (u32)(tau << 5)) + ks * 4096;
          u32x2 a0 = ldstr(a), a1 = ldstr(a + 1024);
          FW[s & 1][ks][tau] = __builtin_bit_cast(bf16x8, (u32x4){a0[0], a0[1], a1[0], a1[1]});
        }
      };
      bar();
      STAMP(4);
#pragma unroll
      for (int ks = 0; ks < 4; ks++) rdf(0, ks);
#pragma unroll
      for (int s = 0; s < 8; s++) {
        if (s == 0) waitv<0>();
        if (s == 0) STAMP(11);
        if (s == 1) STAMP(13);
        if (s == 5) STAMP(15);
        if (s == 7) STAMP(18);
        bar();
        if (s == 0) STAMP(12);
        if (s == 1) STAMP(14);
        if (s == 5) STAMP(16);
        if (s == 6) STAMP(17);
        if (s == 1 && tid == 0) st4c(ws + WS_UFLAG, (u32)tb * 4, EP);
#pragma unroll
        for (int ks = 0; ks < 4; ks++) {
          if (s < 7) rdf(s + 1, ks);
#pragma unroll
          for (int tau = 0; tau < 2; tau++)
#pragma unroll
            for (int j = 0; j < 2; j++)
              acc[tau][j] = __builtin_amdgcn_mfma_f32_32x32x16_bf16(FW[s & 1][ks][tau], FX[s & 1][ks][j], acc[tau][j], 0, 0, 0);
          __builtin_amdgcn_sched_barrier(0);
        }
      }
      STAMP(5);
      char* hp = ws + WS_H + ((size_t)bm * 224 + bn * 16 + wf * 8) * 2048;
#pragma unroll
      for (int j = 0; j < 2; j++) {
        f32x2 st = ldsld<f32x2>(L_EV + 1024 + (tl + 32 * j) * 8);
        const f32x2 r2 = {st[0], st[0]}, rm2 = {-st[1], -st[1]};
#pragma unroll
        for (int tau = 0; tau < 2; tau++) {
#pragma unroll
          for (int hf = 0; hf < 2; hf++) {
            u32x4 o;
#pragma unroll
            for (int jj = 0; jj < 4; jj++) {
              f32x4 cb = ldsld<f32x4>(L_EV + (wf * 32 + 16 * hh + 8 * tau + 4 * hf + jj) * 16);
              f32x2 av = {acc[tau][j][8 * hf + 2 * jj], acc[tau][j][8 * hf + 2 * jj + 1]};
              f32x2 h = __builtin_elementwise_fma(r2, av, __builtin_elementwise_fma(rm2, (f32x2){cb[0], cb[1]}, (f32x2){cb[2], cb[3]}));
              f32x2 h2 = h * 1.44269504088896341f;
              f32x2 g1 = (f32x2){__builtin_amdgcn_exp2f(h2[0]), __builtin_amdgcn_exp2f(h2[1])} + 1.f;
              f32x2 dn = __builtin_elementwise_fma(g1, g1, (f32x2){1.f, 1.f});
              f32x2 rc = {__builtin_amdgcn_rcpf(dn[0]), __builtin_amdgcn_rcpf(dn[1])};
              f32x2 ov = __builtin_elementwise_fma(h * rc, (f32x2){-2.f, -2.f}, h);
              o[jj] = pkbf(ov[0], ov[1]);
            }
            st16c(hp, (u32)((4 * hh + 2 * tau + hf) * 2048 + (tl + 32 * j) * 16), o);
          }
        }
      }
      STAMP(6);
      waitv<0>();
      if (lane == 0) st4c(ws + WS_HFLAG, (u32)((bm * 2 + wt) * 32 + bn * 2 + wf) * 4, EP);
      STAMP(7);
    } else {
      waitv<0>();
      float cs[8];
#pragma unroll
      for (int e = 0; e < 8; e++) cs[e] = 0.f;
#pragma unroll
      for (int j = 0; j < 16; j++) {
        u32x4 cw = ldsld<u32x4>((u32)(j * 4096 + tid * 16));
#pragma unroll
        for (int e = 0; e < 4; e++) { cs[2 * e] += bf2f(cw[e]); cs[2 * e + 1] += bfhi(cw[e]); }
      }
      ldsst<f32x4>((u32)(81920 + tid * 32), (f32x4){cs[0], cs[1], cs[2], cs[3]});
      ldsst<f32x4>((u32)(81920 + tid * 32 + 16), (f32x4){cs[4], cs[5], cs[6], cs[7]});
      bar();
      if (w == 0) {
        float s = 0.f;
#pragma unroll 8
        for (int r = 0; r < 32; r++) s += ldsld<float>((u32)(81920 + r * 256 + lane * 4));
        if (ci < 28) st8c(ws + WS_CS + (size_t)ci * 512, (u32)lane * 8, (u32x2){__float_as_uint(s), EP});
      }
#pragma unroll 1
      for (int s = 0; s < 8; s++) {
        bar();
        if (s == 1 && tid == 0) st4c(ws + WS_UFLAG, (u32)tb * 4, EP);
      }
      if (b == 255 && w == 1) {
        u32 spins = 0;
        u32x4 r0, r1;
        for (;;) {
          r0 = ld16c_wait(ws + WS_R, (u32)lane * 32); r1 = ld16c_wait(ws + WS_R, (u32)lane * 32 + 16);
          if (__all(r0[1] == leadN && r0[3] == leadN && r1[1] == leadN && r1[3] == leadN) || ++spins > SPIN_MAX) break;
        }
        st16c(ws + WS_A, (u32)lane * 16, (u32x4){r0[0], r0[2], r1[0], r1[2]});
      }
    }
    {
      const int wd = wr & 1, wt2 = wr >> 1;
      const int tl = wt2 * 32 + (lane & 31);
      const u32 ha0 = (u32)(hh * 1024 + tl * 16);
      const u32 wa0 = (u32)((8 * hh + q4) * 128 + (((4 * wd + 2 * (p4 & 1) + blk) ^ ((q4 & 2) << 1)) << 4) + 8 * (p4 >> 1));
      const int dl = wd * 32 + 16 * hh;
      u32x4 xv0, xv1;
      {
        u32 go = (u32)((bm2 * 64 + tl) * 1024 + (bn2 * 64 + dl) * 2);
        asm volatile("global_load_dwordx4 %0, %2, %3\n\tglobal_load_dwordx4 %1, %2, %3 offset:16" : "=&v"(xv0), "=&v"(xv1) : "v"(go), "s"(x) : "memory");
      }
      f32x16 acc;
#pragma unroll
      for (int i = 0; i < 16; i++) acc[i] = 0.f;
      bf16x8 FH[2][8], FW[2][8];
      auto rdf = [&](int s, int ks) {
        FH[s & 1][ks] = ldsld<bf16x8>(hslot(s) + ha0 + ks * 2048);
        u32 a = wslot(s) + wa0 + ks * 2048;
        u32x2 a0 = ldstr(a), a1 = ldstr(a + 512);
        FW[s & 1][ks] = __builtin_bit_cast(bf16x8, (u32x4){a0[0], a0[1], a1[0], a1[1]});
      };
      bar();
      bar();
      STAMP(8);
#pragma unroll
      for (int ks = 0; ks < 8; ks++) rdf(0, ks);
#pragma unroll
      for (int s = 0; s < 14; s++) {
        if (s < 13) bar();
#pragma unroll
        for (int ks = 0; ks < 8; ks++) {
          if (s < 13) rdf(s + 1, ks);
          acc = __builtin_amdgcn_mfma_f32_32x32x16_bf16(FW[s & 1][ks], FH[s & 1][ks], acc, 0, 0, 0);
          __builtin_amdgcn_sched_barrier(0);
        }
      }
      STAMP(9);
      asm volatile("s_waitcnt vmcnt(0)" : "+v"(xv0), "+v"(xv1)::"memory");
      u32 ya = L_SPARE + (u32)((dl + q4) * 128 + (p4 >> 1) * 64 + (wt2 * 2 + blk) * 16 + (p4 & 1) * 8);
#pragma unroll
      for (int hf = 0; hf < 2; hf++) {
        u32x2 y0 = ldstr(ya + hf * 1024), y1 = ldstr(ya + hf * 1024 + 512);
        u32x4 yy = {y0[0], y0[1], y1[0], y1[1]};
        u32x4 xv = hf ? xv1 : xv0;
        u32x4 ov;
#pragma unroll
        for (int j = 0; j < 4; j++) {
          u32 s1 = pkbf(bf2f(xv[j]) + bf2f(yy[j]), bfhi(xv[j]) + bfhi(yy[j]));
          u32 s2 = pkbf(acc[8 * hf + 2 * j], acc[8 * hf + 2 * j + 1]);
          ov[j] = pkbf(bf2f(s1) + bf2f(s2), bfhi(s1) + bfhi(s2));
        }
        ldsst<u32x4>(L_SPARE + 8192 + (u32)(tl * 128 + (((4 * wd + 2 * hh + hf) ^ (tl & 7)) << 4)), ov);
      }
    }
  } else {
    const int bt = tid - 256;
    float kinv0, kinv1;
    u32 PK[2][5], QK[2][5];
    if (isg1) waitv<10>();
    bar();
    if (isg1) issue1(2);
    {
      u32 ad[11];
#pragma unroll
      for (int e = 0; e < 11; e++) {
        int lag = 2047 - 8 * bt - e;
        u32 u = __float_as_uint((float)lag);
        u32 id = ((u + 0x7fffu + ((u >> 16) & 1u)) >> 16) - 0x4300u + 128u;
        id = lag < 128 ? (u32)(lag < 0 ? 0 : lag) : id;
        ad[e] = L_KREG + 4096 + id * 4 + (id >= 384u ? 16384u : 0u);
      }
#pragma unroll
      for (int ch = 0; ch < 2; ch++) {
        float bnd = ldsld<float>(L_KREG + ch * 32768 + 4096 + 2816) + ldsld<float>(L_KREG + ch * 32768 + 8192 + 4096 + 2816);
        float ks, ki;
        pow2scale(bnd, 14, ks, ki);
        if (ch) kinv1 = ki; else kinv0 = ki;
        float v[11];
#pragma unroll
        for (int e = 0; e < 11; e++) {
          float t = ldsld<float>(ad[e] + ch * 32768) + ldsld<float>(ad[e] + ch * 32768 + 8192);
          v[e] = (e >= 8 && bt == 255) ? 0.f : t * ks;
        }
#pragma unroll
        for (int j = 0; j < 5; j++) { PK[ch][j] = pkh(v[2 * j], v[2 * j + 1]); QK[ch][j] = pkh(v[2 * j + 1], v[2 * j + 2]); }
      }
    }
    STAMP(11);
    if (isg1) waitv<10>();
    bar();
    STAMP(12);
    if (isg1) issue1(3);
#pragma unroll
    for (int ch = 0; ch < 2; ch++) {
      u32 a = L_KCP + ch * 16640 + bt * 16;
      ldsst<u32x4>(a, (u32x4){PK[ch][0], PK[ch][1], PK[ch][2], PK[ch][3]});
      ldsst<u32x4>(a + 4160, (u32x4){QK[ch][0], QK[ch][1], QK[ch][2], QK[ch][3]});
      ldsst<u32x4>(a + 8320, (u32x4){PK[ch][1], PK[ch][2], PK[ch][3], PK[ch][4]});
      ldsst<u32x4>(a + 12480, (u32x4){QK[ch][1], QK[ch][2], QK[ch][3], QK[ch][4]});
    }
    if (bt < 16) ldsst<u32x4>(L_KCP + (bt >> 1) * 4160 + (256 + (bt & 1)) * 16, (u32x4){0, 0, 0, 0});
    if (lane < 32) ldsst<u32x4>(L_UL + wr * 4608 + lane * 16, (u32x4){0, 0, 0, 0});
    if (isg1) waitv<8>(); else waitv<0>();
    STAMP(13);
    bar();
    STAMP(14);
#pragma unroll
    for (int s = 3; s <= 5; s++) {
      if (isg1) { issue1(s + 1); waitv<8>(); }
      bar();
    }
    if (isg1) issue1(7);
    const u32 rb_ = a_old + 1;
    auto handshake = [&]() {
      u32 spins = 0, nprev = 0; bool first = true;
      for (;;) {
        if (wr == 0) {
          u32 n = ld4c_wait(ws + WS_N, 0);
          if (first || n != nprev) { if (lane == 0) st8c(ws + WS_R, (u32)b * 8, (u32x2){rb_, n}); nprev = n; first = false; }
        }
        u32 a = ld4c_wait(ws + WS_A, (u32)b * 4);
        if (__all(a == rb_) || ++spins > SPIN_MAX) break;
      }
    };
    if (isg1) handshake();
    u32x2 gv;
    u32x4 uf;
    u32 bv;
    const void* gbase = wr < 2 ? (const void*)(ws + WS_STAT + (size_t)bm * 1024) : (const void*)(ws + WS_CS + (size_t)bn * 1024 - 1024);
    {
      int bidx = (isg1 ? bn * 128 : 0) + (bt & 127);
      asm volatile("global_load_dwordx2 %0, %3, %6 sc1\n\tglobal_load_dwordx4 %1, %4, %7 sc1\n\tglobal_load_ushort %2, %5, %8"
                   : "=&v"(gv), "=&v"(uf), "=&v"(bv)
                   : "v"((u32)bt * 8), "v"((u32)lane * 16), "v"((u32)bidx * 2), "s"(isg1 ? gbase : (const void*)ws), "s"(ws + WS_UFLAG), "s"(b1)
                   : "memory");
    }
    STAMP(15);
    waitv<11>();
    bar();
    STAMP(16);
    waitv<3>();
    bar();
    STAMP(17);
    asm volatile("s_waitcnt vmcnt(0)" : "+v"(gv), "+v"(uf), "+v"(bv)::"memory");
    if (isg1) {
      u32 spins = 0;
      while (!__all(gv[1] == EP) && ++spins < SPIN_MAX) gv = ld8c_wait(gbase, (u32)bt * 8);
      if (wr < 2) {
        float m = bf2f(gv[0]), sd = bfhi(gv[0]);
        float r = 1.f / sd;
        ldsst<f32x2>(L_EV + 1024 + bt * 8, (f32x2){r, r * m});
      } else {
        int f = bt - 128;
        ldsst<float>(L_EV + (f >> 1) * 16 + (f & 1) * 4, __uint_as_float(gv[0]));
        ldsst<float>(L_EV + (f >> 1) * 16 + (f & 1) * 4 + 8, bf2f(bv));
      }
    }
    STAMP(18);
    bar();
    STAMP(4);
    if (!isg1) { handshake(); uf = ld16c_wait(ws + WS_UFLAG, (u32)lane * 16); }
    {
      u32 spins = 0;
      while (!__all(uf[0] == EP && uf[1] == EP && uf[2] == EP && uf[3] == EP) && ++spins < SPIN_MAX)
        uf = ld16c_wait(ws + WS_UFLAG, (u32)lane * 16);
    }
    const int ch = wr >> 1, cset = wr & 1;
    {
      u32 vo = (u32)(lane * 8192 + ch * 16);
      glds4<1024, true>(ws + WS_U + (size_t)tb * 32, vo, vo + 524288, vo + 1048576, vo + 1572864, L_UL + wr * 4608 + 512);
    }
    u32 voW2;
    {
      int row = 32 * wr + (lane >> 3);
      voW2 = (u32)(row * 1024 + (((lane & 7) ^ ((row & 2) << 1)) << 4));
    }
    const char* w2b = (const char*)w2 + bn2 * 128;
    auto issue2w = [&](int s, u32 slot) {
      int kt = s + rot2; kt = kt >= 14 ? kt - 14 : kt;
      glds4<1024, false>(w2b + (size_t)kt * 131072, voW2, voW2 + 8192, voW2 + 16384, voW2 + 24576, slot + wr * 4096);
    };
    issue2w(0, wslot(0));
    u32 dskv = Dsk[2 * tb + ch];
    waitv<4>();
    STAMP(5);
    {
      const int n = lane & 15, kg = lane >> 4;
      f32x4 acc[4];
#pragma unroll
      for (int a = 0; a < 4; a++) acc[a] = (f32x4){0.f, 0.f, 0.f, 0.f};
      const int cc = (3 - n) & 3;
      u32 ub = L_UL + wr * 4608 + (u32)((256 + 16 * n - 16 + 8 * kg) * 2);
      u32 ab = L_KCP + ch * 16640 + cc * 4160 + (u32)((2031 - n + 8 * kg - cc - 1792) * 2);
#pragma unroll 1
      for (int it = 0; it < 8; it++) {
        if (it >= 1 && it <= 4) issue2w(it, (u32)it * 16384u);
        if (cset) conv_body<1>(acc, ub, ab); else conv_body<0>(acc, ub, ab);
        ub -= 64; ab -= 64;
      }
      STAMP(6);
      const float dsk = bf2f(dskv), kinv = ch ? kinv1 : kinv0;
      char* yp = ws + WS_Y + (size_t)(2 * tb + ch) * 4096;
#pragma unroll
      for (int pr = 0; pr < 2; pr++) {
        u32 pk[2][2];
#pragma unroll
        for (int j = 0; j < 2; j++) {
          int a = cset ? (pr ? 5 + j : 1 + j) : (pr ? 4 + 3 * j : 3 * j);
          int t = 256 * a + 16 * n + 4 * kg;
          f16x4 uu = ldsld<f16x4>(L_UL + wr * 4608 + 512 + t * 2);
          f32x4 yy = acc[2 * pr + j];
          u32 y01 = pkbf(yy[0] * kinv, yy[1] * kinv), y23 = pkbf(yy[2] * kinv, yy[3] * kinv);
          u32 s01 = pkbf((float)uu[0] * dsk, (float)uu[1] * dsk), s23 = pkbf((float)uu[2] * dsk, (float)uu[3] * dsk);
          pk[j][0] = pkbf(bf2f(y01) + bf2f(s01), bfhi(y01) + bfhi(s01));
          pk[j][1] = pkbf(bf2f(y23) + bf2f(s23), bfhi(y23) + bfhi(s23));
        }
        auto s0 = __builtin_amdgcn_permlane16_swap(pk[0][0], pk[1][0], false, false);
        auto s1 = __builtin_amdgcn_permlane16_swap(pk[0][1], pk[1][1], false, false);
        int a0 = cset ? (pr ? 5 : 1) : (pr ? 4 : 0), a1 = cset ? (pr ? 6 : 2) : (pr ? 7 : 3);
        int as = (kg & 1) ? a1 : a0;
        st16c(yp, (u32)(as * 512 + (kg >> 1) * 256 + n * 16), (u32x4){s0[0], s1[0], s0[1], s1[1]});
      }
    }
    waitv<0>();
    if (lane == 0) st4c(ws + WS_YFLAG, (u32)(cset * 512 + 2 * tb + ch) * 4, EP);
    STAMP(7);
    {
      u32 spins = 0;
      for (;;) {
        u32 f = ld4c_wait(ws + WS_HFLAG + (size_t)bm2 * 128, (u32)(lane < 28 ? lane : 0) * 4);
        if (__all(f == EP) || ++spins > SPIN_MAX) break;
      }
    }
    STAMP(8);
    const char* hb = ws + WS_H + (size_t)(bm2 >> 1) * 224 * 2048 + (bm2 & 1) * 1024 + wr * 8192;
    const u32 voH = (u32)lane * 16;
    auto issue2h = [&](int s) {
      int kt = s + rot2; kt = kt >= 14 ? kt - 14 : kt;
      glds4<1024, true>(hb + (size_t)kt * 32768, voH, voH + 2048, voH + 4096, voH + 6144, hslot(s) + wr * 4096);
    };
    bar();
    issue2h(0); issue2h(1); issue2h(2); issue2h(3);
    u32 yf;
    const int ya_ = bm2 >> 2;
    const int yset = (ya_ == 1 || ya_ == 2 || ya_ == 5 || ya_ == 6) ? 1 : 0;
    const void* yfb = ws + WS_YFLAG + (size_t)(yset * 512 + bn2 * 64) * 4;
    auto step = [&](auto S_) {
      constexpr int j = decltype(S_)::value;
      waitv<g2_wait(j)>();
      bar();
      if constexpr (j >= 1) {
        if constexpr (j + 3 <= 13) issue2h(j + 3);
        if constexpr (j + 4 <= 13) issue2w(j + 4, wslot(j + 4));
        if constexpr (j == 5)
          asm volatile("global_load_dword %0, %1, %2 sc1" : "=&v"(yf) : "v"((u32)lane * 4), "s"(yfb) : "memory");
        if constexpr (j == 9) {
          asm volatile("" : "+v"(yf)::"memory");
          u32 spins = 0;
          while (!__all(yf == EP) && ++spins < SPIN_MAX) yf = ld4c_wait(yfb, (u32)lane * 4);
          const char* yb = ws + WS_Y + (size_t)bn2 * 64 * 4096 + ya_ * 512 + (bm2 & 3) * 64;
          u32 vo = (u32)((16 * wr + (lane >> 3)) * 4096 + ((lane >> 2) & 1) * 256 + (lane & 3) * 16);
          glds1c(yb, vo, L_SPARE + wr * 2048);
          glds1c(yb, vo + 32768, L_SPARE + wr * 2048 + 1024);
        }
      }
    };
#define G2S(n) step(std::integral_constant<int, n>{});
    G2S(0) G2S(1) G2S(2) G2S(3) G2S(4) G2S(5) G2S(6) G2S(7) G2S(8) G2S(9) G2S(10) G2S(11) G2S(12) G2S(13)
    STAMP(9);
  }
  bar();
  {
    int t = tid >> 3, c = tid & 7;
    u32x4 ov = ldsld<u32x4>(L_SPARE + 8192 + (u32)(t * 128 + ((c ^ (t & 7)) << 4)));
    st16((char*)out + (size_t)bm2 * 65536 + bn2 * 128, (u32)(t * 1024 + c * 16), ov);
  }
  STAMP(10);
}

extern "C" void kernel_launch(void* const* d_in, const int* in_sizes, int n_in, void* d_out, int out_size, void* d_ws,
                              size_t ws_size, hipStream_t stream) {
  fused_kernel<<<256, 512, 0, stream>>>((const u16*)d_in[0], (const u16*)d_in[1], (const u16*)d_in[2], (const u16*)d_in[3],
                                        (const u16*)d_in[4], (const u16*)d_in[5], (const u16*)d_in[6], (const u16*)d_in[7],
                                        (const u16*)d_in[8], (const u16*)d_in[10], (const u16*)d_in[11], (const u16*)d_in[12],
                                        (u16*)d_out, (char*)d_ws);
}
```

```cpp
#include <hip/hip_runtime.h>
#include <stdint.h>
#include <type_traits>

typedef unsigned short u16;
typedef unsigned int u32;
typedef unsigned long long u64;
typedef __attribute__((ext_vector_type(8))) __bf16 bf16x8;
typedef __attribute__((ext_vector_type(2))) __bf16 bf16x2;
typedef __attribute__((ext_vector_type(8))) _Float16 f16x8;
typedef __attribute__((ext_vector_type(4))) _Float16 f16x4;
typedef __attribute__((ext_vector_type(2))) _Float16 f16x2;
typedef __attribute__((ext_vector_type(2))) float f32x2;
typedef __attribute__((ext_vector_type(4))) float f32x4;
typedef __attribute__((ext_vector_type(16))) float f32x16;
typedef __attribute__((ext_vector_type(4))) u32 u32x4;
typedef __attribute__((ext_vector_type(2))) u32 u32x2;
typedef __attribute__((ext_vector_type(4))) short s16x4;
#define AS3 __attribute__((address_space(3)))
#define DEVI __device__ __forceinline__
template <int N> using IC = std::integral_constant<int, N>;

constexpr u32 WS_STAT = 0;
constexpr u32 WS_CS = 16384;
constexpr u32 WS_UFLAG = 30720;
constexpr u32 WS_YFLAG = 31744;
constexpr u32 WS_HFLAG = 35840;
constexpr u32 WS_R = 40960;
constexpr u32 WS_A = 45056;
constexpr u32 WS_N = 47104;
constexpr size_t WS_U = 1u << 20, WS_Y = 4u << 20, WS_H = 8u << 20, WS_DBG = 32u << 20;
constexpr u32 LA_XA = 65536, LA_WA = 67584, LA_RS = 69632, LA_ZERO = 70144;
constexpr u32 LA_YT = 65536, LA_OUT = 73728;
constexpr u32 LB_TAB = 0;
constexpr u32 LB_KCP = 0;
constexpr u32 LB_RED = 33280;
constexpr u32 LB_UL = 65536;
constexpr u32 LB_LNS = 65536;
constexpr u32 LB_ST = 74752;
constexpr u32 SPIN_MAX = 1u << 15;
constexpr u32 EP = 1u;

template <typename T> DEVI T ldsld(u32 a) { return *(AS3 T*)(uintptr_t)a; }
template <typename T> DEVI T ldsldv(u32 a) { return *(volatile AS3 T*)(uintptr_t)a; }
template <typename T> DEVI void ldsst(u32 a, T v) { *(AS3 T*)(uintptr_t)a = v; }
DEVI u32x2 ldstr(u32 a) { return __builtin_bit_cast(u32x2, __builtin_amdgcn_ds_read_tr16_b64_v4i16((AS3 s16x4*)(uintptr_t)a)); }
DEVI float bf2f(u32 v) { return __uint_as_float(v << 16); }
DEVI float bfhi(u32 v) { return __uint_as_float(v & 0xffff0000u); }
DEVI float rbf(float f) { return (float)(__bf16)f; }
DEVI u32 pkbf(float a, float b) { bf16x2 v = {(__bf16)a, (__bf16)b}; return __builtin_bit_cast(u32, v); }
DEVI u32 pkh(float a, float b) { f16x2 v = {(_Float16)a, (_Float16)b}; return __builtin_bit_cast(u32, v); }

template <int STEP, bool COH>
DEVI void glds2(const void* sbase, u32 v0, u32 v1, u32 lds0) {
  u32 keep;
  if (COH)
    asm volatile("s_mov_b32 %0, m0\n\ts_mov_b32 m0, %4\n\ts_nop 0\n\tglobal_load_lds_dwordx4 %1, %3 sc1\n\t"
                 "s_add_u32 m0, m0, %5\n\ts_nop 0\n\tglobal_load_lds_dwordx4 %2, %3 sc1\n\ts_mov_b32 m0, %0"
                 : "=&s"(keep) : "v"(v0), "v"(v1), "s"(sbase), "s"(lds0), "n"(STEP) : "memory", "scc");
  else
    asm volatile("s_mov_b32 %0, m0\n\ts_mov_b32 m0, %4\n\ts_nop 0\n\tglobal_load_lds_dwordx4 %1, %3\n\t"
                 "s_add_u32 m0, m0, %5\n\ts_nop 0\n\tglobal_load_lds_dwordx4 %2, %3\n\ts_mov_b32 m0, %0"
                 : "=&s"(keep) : "v"(v0), "v"(v1), "s"(sbase), "s"(lds0), "n"(STEP) : "memory", "scc");
}
template <int SSTEP, bool COH>
DEVI void glds8(const char* sbase, u32 v0, u32 v1, u32 lds0) {
#pragma unroll
  for (int j = 0; j < 4; j++) glds2<1024, COH>(sbase + (size_t)j * SSTEP, v0, v1, lds0 + j * 2048);
}
DEVI void st16c(void* sbase, u32 voff, u32x4 v) {
  asm volatile("global_store_dwordx4 %0, %1, %2 sc1\n\ts_nop 1" ::"v"(voff), "v"(v), "s"(sbase) : "memory");
}
DEVI void st16(void* sbase, u32 voff, u32x4 v) {
  asm volatile("global_store_dwordx4 %0, %1, %2\n\ts_nop 1" ::"v"(voff), "v"(v), "s"(sbase) : "memory");
}
DEVI void st8c(void* sbase, u32 voff, u32x2 v) {
  asm volatile("global_store_dwordx2 %0, %1, %2 sc1\n\ts_nop 1" ::"v"(voff), "v"(v), "s"(sbase) : "memory");
}
DEVI void st4c(void* sbase, u32 voff, u32 v) {
  asm volatile("global_store_dword %0, %1, %2 sc1\n\ts_nop 1" ::"v"(voff), "v"(v), "s"(sbase) : "memory");
}
DEVI u32 ld4c_wait(const void* sbase, u32 voff) {
  u32 r;
  asm volatile("global_load_dword %0, %1, %2 sc1\n\ts_waitcnt vmcnt(0)" : "=&v"(r) : "v"(voff), "s"(sbase) : "memory");
  return r;
}
DEVI u32x4 ld16c_wait(const void* sbase, u32 voff) {
  u32x4 r;
  asm volatile("global_load_dwordx4 %0, %1, %2 sc1\n\ts_waitcnt vmcnt(0)" : "=&v"(r) : "v"(voff), "s"(sbase) : "memory");
  return r;
}
template <int N> DEVI void waitv() { asm volatile("s_waitcnt vmcnt(%0)" ::"n"(N) : "memory"); }
DEVI void bar() {
  asm volatile("s_waitcnt lgkmcnt(0)" ::: "memory");
  __builtin_amdgcn_s_barrier();
  asm volatile("" ::: "memory");
}
template <int CTRL, int RM> DEVI float dppf(float old, float v) {
  return __int_as_float(__builtin_amdgcn_update_dpp(__float_as_int(old), __float_as_int(v), CTRL, RM, 0xf, false));
}
DEVI float wave_sum(float v) {
  v += dppf<0x111, 0xf>(0.f, v); v += dppf<0x112, 0xf>(0.f, v); v += dppf<0x114, 0xf>(0.f, v); v += dppf<0x118, 0xf>(0.f, v);
  v += dppf<0x142, 0xa>(0.f, v); v += dppf<0x143, 0xc>(0.f, v);
  return __int_as_float(__builtin_amdgcn_readlane(__float_as_int(v), 63));
}
DEVI float wave_max0(float v) {
  v = fmaxf(v, dppf<0x111, 0xf>(0.f, v)); v = fmaxf(v, dppf<0x112, 0xf>(0.f, v)); v = fmaxf(v, dppf<0x114, 0xf>(0.f, v));
  v = fmaxf(v, dppf<0x118, 0xf>(0.f, v)); v = fmaxf(v, dppf<0x142, 0xa>(0.f, v)); v = fmaxf(v, dppf<0x143, 0xc>(0.f, v));
  return __int_as_float(__builtin_amdgcn_readlane(__float_as_int(v), 63));
}
#define CM_MUL(t, a, b) "v_pk_mul_f32 " t ", " a ", " b " op_sel_hi:[0,1]\n\t"
#define CM_FMA(r, a, b, t) "v_pk_fma_f32 " r ", " a ", " b ", " t " op_sel:[1,1,0] op_sel_hi:[1,0,1] neg_lo:[1,0,0]\n\t"
DEVI f32x2 cmul(f32x2 a, f32x2 b) {
  f32x2 t, r;
  asm(CM_MUL("%0", "%1", "%2") : "=v"(t) : "v"(a), "v"(b));
  asm(CM_FMA("%0", "%1", "%2", "%3") : "=v"(r) : "v"(a), "v"(b), "v"(t));
  return r;
}
DEVI void cmul4(f32x2& a0, f32x2& a1, f32x2& a2, f32x2& a3, f32x2 z) {
  f32x2 t0, t1, t2, t3;
  asm(CM_MUL("%0", "%4", "%8") CM_MUL("%1", "%5", "%8") CM_MUL("%2", "%6", "%8") CM_MUL("%3", "%7", "%8")
      CM_FMA("%0", "%4", "%8", "%0") CM_FMA("%1", "%5", "%8", "%1") CM_FMA("%2", "%6", "%8", "%2") CM_FMA("%3", "%7", "%8", "%3")
      : "=&v"(t0), "=&v"(t1), "=&v"(t2), "=&v"(t3) : "v"(a0), "v"(a1), "v"(a2), "v"(a3), "v"(z));
  a0 = t0; a1 = t1; a2 = t2; a3 = t3;
}
DEVI f32x2 cpow(float a2, float fhi, float flo, float R) {
  float m = __builtin_amdgcn_exp2f(a2 * R);
  float ph = __builtin_fmaf(flo, R, __builtin_amdgcn_fractf(fhi * R));
  return (f32x2){m * __builtin_amdgcn_cosf(ph), m * __builtin_amdgcn_sinf(ph)};
}
DEVI void pow2scale(float mx, int target, float& s, float& inv) {
  int ex = (int)((__float_as_uint(mx) >> 23) & 0xff);
  int se = 254 + target - ex;
  se = se < 2 ? 2 : (se > 252 ? 252 : se);
  s = __uint_as_float((u32)se << 23);
  inv = __uint_as_float((u32)(254 - se) << 23);
}

struct Shake {
  u32 rb, nsent;
  bool sent;
};
DEVI void shake_poll(char* ws, u32 id) {
  asm volatile("global_load_dword v126, %0, %2 sc1\n\tglobal_load_dword v127, %1, %2 sc1"
               :: "v"(WS_N), "v"(WS_A + id * 4), "s"(ws) : "memory", "v126", "v127");
}
DEVI bool shake_step(Shake& S, char* ws, u32 id, int lane) {
  u32 n, a;
  asm volatile("v_readfirstlane_b32 %0, v126\n\tv_readfirstlane_b32 %1, v127" : "=s"(n), "=s"(a)::"memory");
  if (a == S.rb) return true;
  if (!S.sent || n != S.nsent) {
    if (lane == 0) st8c(ws, WS_R + id * 8, (u32x2){S.rb, n});
    S.sent = true; S.nsent = n;
  }
  shake_poll(ws, id);
  return false;
}
DEVI void shake_init(Shake& S, char* ws, u32 id) {
  shake_poll(ws, id);
  u32 a;
  asm volatile("s_waitcnt vmcnt(0)\n\tv_readfirstlane_b32 %0, v127" : "=s"(a)::"memory");
  S.rb = a + 1u;
  S.sent = false; S.nsent = 0;
}
DEVI void shake_finish(Shake& S, char* ws, u32 id, int lane) {
  u32 spins = 0;
  while (!shake_step(S, ws, id, lane) && ++spins < SPIN_MAX) waitv<0>();
  waitv<0>();
}

__global__ __launch_bounds__(512, 4) void fused_kernel(
    const u16* __restrict__ x, const u16* __restrict__ Are, const u16* __restrict__ Aim, const u16* __restrict__ Bre,
    const u16* __restrict__ Bim, const u16* __restrict__ Cre, const u16* __restrict__ Cim, const u16* __restrict__ ldt,
    const u16* __restrict__ Dsk, const u16* __restrict__ w1, const u16* __restrict__ b1, const u16* __restrict__ w2,
    u16* __restrict__ out, char* __restrict__ ws) {
  __shared__ __attribute__((aligned(16))) char sm[81920];
  asm volatile("" ::"v"(sm) : "memory");
  const int tid = threadIdx.x, lane = tid & 63;
  const int w = __builtin_amdgcn_readfirstlane(tid >> 6);
  const int gb = blockIdx.x, b = gb & 255, xcd = b & 7, bi = b >> 3;
  const bool isg1 = bi < 28;
  const int bm = 2 * xcd + (bi & 1), bn = bi >> 1;
  const int tb = 32 * xcd + bi;
#define STAMP(i)
#define STAMP_OUT()
  STAMP(0);
  if (gb < 256) {
    const int bm2 = 4 * xcd + (bi & 3), bn2 = bi >> 2;
    const int rot2 = (bi * 9) % 28;
    const int ci = xcd * 4 + (bi - 28);
    if (w < 4) {
      __builtin_amdgcn_s_setprio(2);
      const int hh = lane >> 5, blk = (lane >> 4) & 1, q4 = (lane & 15) >> 2, p4 = lane & 3;
      if (isg1) {
        const int wf = w & 1, wt = w >> 1;
        const int tl = wt * 64 + (lane & 31);
        const u32 xa0 = (u32)(tl * 64 + ((hh ^ ((tl >> 2) & 3)) << 4));
        const u32 wa0 = (u32)(8192 + (8 * hh + q4) * 256 +
                              (((8 * wf + 4 * (p4 & 1) + blk) ^ (((q4 & 1) << 1) | ((q4 & 2) << 2))) << 4) + 8 * (p4 >> 1));
        f32x16 acc[2][2];
#pragma unroll
        for (int i = 0; i < 16; i++) { acc[0][0][i] = 0.f; acc[0][1][i] = 0.f; acc[1][0][i] = 0.f; acc[1][1][i] = 0.f; }
        bf16x8 FX[2][2], FW[2][2];
        auto rdf = [&](u32 sb, auto KS_) {
          constexpr int ks = decltype(KS_)::value;
#pragma unroll
          for (int j = 0; j < 2; j++) FX[ks][j] = ldsld<bf16x8>(sb + (xa0 ^ (u32)(ks << 5)) + j * 2048);
#pragma unroll
          for (int tau = 0; tau < 2; tau++) {
            u32 a = sb + (wa0 ^ (u32)(tau << 5)) + ks * 4096;
            u32x2 a0 = ldstr(a), a1 = ldstr(a + 1024);
            FW[ks][tau] = __builtin_bit_cast(bf16x8, (u32x4){a0[0], a0[1], a1[0], a1[1]});
          }
        };
        auto mm = [&](auto KS_) {
          constexpr int ks = decltype(KS_)::value;
#pragma unroll
          for (int tau = 0; tau < 2; tau++)
#pragma unroll
            for (int j = 0; j < 2; j++)
              acc[tau][j] = __builtin_amdgcn_mfma_f32_32x32x16_bf16(FW[ks][tau], FX[ks][j], acc[tau][j], 0, 0, 0);
        };
        bar();
        STAMP(1);
        rdf(0, IC<0>{});
#pragma unroll 1
        for (int s = 0; s < 16; s++) {
          rdf((u32)((s & 3) * 16384), IC<1>{});
          __builtin_amdgcn_sched_barrier(0);
          mm(IC<0>{});
          __builtin_amdgcn_sched_barrier(0);
          bar();
          rdf((u32)(((s + 1) & 3) * 16384), IC<0>{});
          __builtin_amdgcn_sched_barrier(0);
          mm(IC<1>{});
          __builtin_amdgcn_sched_barrier(0);
        }
        STAMP(2);
        {
          const int li = lane & 15;
          const u32 fo = (u32)(wf * 64 + 32 * ((li >> 2) & 1) + 8 * blk + 4 * (li >> 3) + (li & 3));
          bf16x8 xa[2], wa[2];
#pragma unroll
          for (int j = 0; j < 2; j++) xa[j] = ldsld<bf16x8>(hh ? LA_ZERO : LA_XA + (tl + 32 * j) * 16);
#pragma unroll
          for (int tau = 0; tau < 2; tau++) wa[tau] = ldsld<bf16x8>(hh ? LA_ZERO : LA_WA + (fo + 16 * tau) * 16);
#pragma unroll
          for (int tau = 0; tau < 2; tau++)
#pragma unroll
            for (int j = 0; j < 2; j++) acc[tau][j] = __builtin_amdgcn_mfma_f32_32x32x16_bf16(wa[tau], xa[j], acc[tau][j], 0, 0, 0);
        }
        char* hp = ws + WS_H + ((size_t)bm * 224 + bn * 16 + wf * 8) * 2048;
#pragma unroll
        for (int j = 0; j < 2; j++) {
          const float r = ldsld<float>(LA_RS + (tl + 32 * j) * 4);
          const float c1 = r * 1.44269504088896341f, m2r = -2.f * r;
          const f32x2 c1v = {c1, c1}, rv = {r, r}, m2v = {m2r, m2r}, one = {1.f, 1.f};
#pragma unroll
          for (int tau = 0; tau < 2; tau++) {
#pragma unroll
            for (int hf = 0; hf < 2; hf++) {
              f32x2 av[4], t[4];
#pragma unroll
              for (int jj = 0; jj < 4; jj++) { av[jj] = (f32x2){acc[tau][j][8 * hf + 2 * jj], acc[tau][j][8 * hf + 2 * jj + 1]}; t[jj] = av[jj] * c1v; }
#pragma unroll
              for (int jj = 0; jj < 4; jj++) t[jj] = (f32x2){__builtin_amdgcn_exp2f(t[jj][0]), __builtin_amdgcn_exp2f(t[jj][1])};
#pragma unroll
              for (int jj = 0; jj < 4; jj++) t[jj] = t[jj] + one;
#pragma unroll
              for (int jj = 0; jj < 4; jj++) t[jj] = __builtin_elementwise_fma(t[jj], t[jj], one);
#pragma unroll
              for (int jj = 0; jj < 4; jj++) t[jj] = (f32x2){__builtin_amdgcn_rcpf(t[jj][0]), __builtin_amdgcn_rcpf(t[jj][1])};
#pragma unroll
              for (int jj = 0; jj < 4; jj++) t[jj] = __builtin_elementwise_fma(t[jj], m2v, rv);
              u32x4 o;
#pragma unroll
              for (int jj = 0; jj < 4; jj++) { f32x2 ov = av[jj] * t[jj]; o[jj] = pkbf(ov[0], ov[1]); }
              st16c(hp, (u32)((4 * hh + 2 * tau + hf) * 2048 + (tl + 32 * j) * 16), o);
            }
          }
        }
        STAMP(3);
        waitv<0>();
        if (lane == 0) st4c(ws, WS_HFLAG + (u32)((bm * 2 + wt) * 32 + bn * 2 + wf) * 4, EP);
        STAMP(4);
      } else {
        u32 leadN = 0;
        if (ci < 28) {
          u32 vo = (u32)((16 * w + (lane >> 3)) * 3584 + (lane & 7) * 16);
          const char* cb = (const char*)w1 + ci * 128;
#pragma unroll 1
          for (int j = 0; j < 8; j++) glds2<1024, false>(cb + (size_t)j * 64 * 3584, vo, vo + 8 * 3584, (u32)(j * 8192 + w * 2048));
          waitv<0>();
          float cs[8];
#pragma unroll
          for (int e = 0; e < 8; e++) cs[e] = 0.f;
#pragma unroll 4
          for (int j = 0; j < 16; j++) {
            u32x4 cw = ldsld<u32x4>((u32)((j >> 1) * 8192 + w * 2048 + (j & 1) * 1024 + lane * 16));
#pragma unroll
            for (int e = 0; e < 4; e++) { cs[2 * e] += bf2f(cw[e]); cs[2 * e + 1] += bfhi(cw[e]); }
          }
          ldsst<f32x4>((u32)(65536 + tid * 32), (f32x4){cs[0], cs[1], cs[2], cs[3]});
          ldsst<f32x4>((u32)(65536 + tid * 32 + 16), (f32x4){cs[4], cs[5], cs[6], cs[7]});
        } else if (ci == 31 && w == 1) {
          u32 cand;
          u32x4 r0, r1, r2, r3;
          asm volatile("global_load_dwordx4 %0, %5, %6 sc1\n\tglobal_load_dwordx4 %1, %5, %6 offset:16 sc1\n\t"
                       "global_load_dwordx4 %2, %5, %6 offset:32 sc1\n\tglobal_load_dwordx4 %3, %5, %6 offset:48 sc1\n\t"
                       "global_load_dword %4, %7, %8 sc1\n\ts_waitcnt vmcnt(0)"
                       : "=&v"(r0), "=&v"(r1), "=&v"(r2), "=&v"(r3), "=&v"(cand) : "v"((u32)lane * 64), "s"(ws + WS_R), "v"(WS_N), "s"(ws) : "memory");
          cand += 1u;
          for (;;) {
            bool hit = r0[1] == cand || r0[3] == cand || r1[1] == cand || r1[3] == cand || r2[1] == cand || r2[3] == cand ||
                       r3[1] == cand || r3[3] == cand;
            if (!__any(hit)) break;
            cand++;
          }
          if (lane == 0) st4c(ws, WS_N, cand);
          leadN = cand;
        }
        bar();
        if (ci < 28 && w == 0) {
          float s = 0.f;
#pragma unroll 8
          for (int r = 0; r < 32; r++) s += ldsld<float>((u32)(65536 + r * 256 + lane * 4));
          st8c(ws, WS_CS + (u32)(ci * 512 + lane * 8), (u32x2){__float_as_uint(s), EP});
        }
        if (ci == 31 && w == 1) {
          u32 spins = 0;
          u32x4 r0, r1, r2, r3;
          for (;;) {
            asm volatile("global_load_dwordx4 %0, %4, %5 sc1\n\tglobal_load_dwordx4 %1, %4, %5 offset:16 sc1\n\t"
                         "global_load_dwordx4 %2, %4, %5 offset:32 sc1\n\tglobal_load_dwordx4 %3, %4, %5 offset:48 sc1\n\ts_waitcnt vmcnt(0)"
                         : "=&v"(r0), "=&v"(r1), "=&v"(r2), "=&v"(r3) : "v"((u32)lane * 64), "s"(ws + WS_R) : "memory");
            bool ok = r0[1] == leadN && r0[3] == leadN && r1[1] == leadN && r1[3] == leadN && r2[1] == leadN && r2[3] == leadN &&
                      r3[1] == leadN && r3[3] == leadN;
            if (__all(ok) || ++spins > SPIN_MAX) break;
          }
          st16c(ws + WS_A, (u32)lane * 32, (u32x4){r0[0], r0[2], r1[0], r1[2]});
          st16c(ws + WS_A, (u32)lane * 32 + 16, (u32x4){r2[0], r2[2], r3[0], r3[2]});
          STAMP(4);
        }
      }
      const int wd = w & 1, wt2 = w >> 1;
      const int tl = wt2 * 32 + (lane & 31);
      const u32 ha0 = (u32)(hh * 1024 + tl * 16);
      const u32 wa0 = (u32)(8192 + (8 * hh + q4) * 128 + (((4 * wd + 2 * (p4 & 1) + blk) ^ ((q4 & 2) << 1)) << 4) + 8 * (p4 >> 1));
      const int dl = wd * 32 + 16 * hh;
      f32x16 acc;
#pragma unroll
      for (int i = 0; i < 16; i++) acc[i] = 0.f;
      bf16x8 FH[4], FW[4];
      auto rdf2 = [&](u32 sb, int ks) {
        FH[ks] = ldsld<bf16x8>(sb + ha0 + ks * 2048);
        u32 a = sb + wa0 + ks * 2048;
        u32x2 a0 = ldstr(a), a1 = ldstr(a + 512);
        FW[ks] = __builtin_bit_cast(bf16x8, (u32x4){a0[0], a0[1], a1[0], a1[1]});
      };
      bar();
      STAMP(5);
      bar();
      STAMP(6);
      rdf2(0, 0); rdf2(0, 1);
#pragma unroll 1
      for (int s = 0; s < 28; s++) {
        const u32 sb = (u32)((s & 3) * 16384), sn = (u32)(((s + 1) & 3) * 16384);
        rdf2(sb, 2); rdf2(sb, 3);
        __builtin_amdgcn_sched_barrier(0);
        acc = __builtin_amdgcn_mfma_f32_32x32x16_bf16(FW[0], FH[0], acc, 0, 0, 0);
        acc = __builtin_amdgcn_mfma_f32_32x32x16_bf16(FW[1], FH[1], acc, 0, 0, 0);
        __builtin_amdgcn_sched_barrier(0);
        bar();
        rdf2(sn, 0); rdf2(sn, 1);
        __builtin_amdgcn_sched_barrier(0);
        acc = __builtin_amdgcn_mfma_f32_32x32x16_bf16(FW[2], FH[2], acc, 0, 0, 0);
        acc = __builtin_amdgcn_mfma_f32_32x32x16_bf16(FW[3], FH[3], acc, 0, 0, 0);
        __builtin_amdgcn_sched_barrier(0);
      }
      STAMP(7);
      u32 ya = LA_YT + (u32)((dl + q4) * 128 + (p4 >> 1) * 64 + (wt2 * 2 + blk) * 16 + (p4 & 1) * 8);
#pragma unroll
      for (int hf = 0; hf < 2; hf++) {
        u32x2 y0 = ldstr(ya + hf * 1024), y1 = ldstr(ya + hf * 1024 + 512);
        u32x4 yy = {y0[0], y0[1], y1[0], y1[1]};
        const u32 oa = LA_OUT + (u32)(tl * 128 + (((4 * wd + 2 * hh + hf) ^ (tl & 7)) << 4));
        u32x4 xv = ldsld<u32x4>(oa);
        u32x4 ov;
#pragma unroll
        for (int j = 0; j < 4; j++) {
          u32 s1 = pkbf(bf2f(xv[j]) + bf2f(yy[j]), bfhi(xv[j]) + bfhi(yy[j]));
          u32 s2 = pkbf(acc[8 * hf + 2 * j], acc[8 * hf + 2 * j + 1]);
          ov[j] = pkbf(bf2f(s1) + bf2f(s2), bfhi(s1) + bfhi(s2));
        }
        ldsst<u32x4>(oa, ov);
      }
      bar();
#pragma unroll
      for (int j = 0; j < 2; j++) {
        int t = (tid >> 3) + 32 * j, c = tid & 7;
        u32x4 ov = ldsld<u32x4>(LA_OUT + (u32)(t * 128 + ((c ^ (t & 7)) << 4)));
        st16((char*)out + (size_t)bm2 * 65536 + bn2 * 128, (u32)(t * 1024 + c * 16), ov);
      }
      STAMP(8);
    } else if (w == 4) {
      if (isg1) {
        const int rot1 = 2 * ((2 * bm + bn) & 7);
        const int row = lane >> 2;
        const u32 vo = (u32)(row * 1024 + (((lane & 3) ^ ((row >> 2) & 3)) << 4));
        const char* xb = (const char*)x + (size_t)bm * 131072;
        auto issue = [&](int s) { glds8<32768, false>(xb + ((s + rot1) & 15) * 64, vo, vo + 16384, (u32)((s & 3) * 16384)); };
        issue(0); issue(1); issue(2); issue(3);
        waitv<24>();
        bar();
#pragma unroll 1
        for (int s = 0; s < 12; s++) { waitv<16>(); bar(); issue(s + 4); }
        waitv<16>(); bar(); waitv<8>(); bar(); waitv<0>(); bar(); bar();
      } else {
        bar();
      }
      const char* hb = ws + WS_H + (size_t)(bm2 >> 1) * 224 * 2048 + (bm2 & 1) * 1024;
      const u32 vo = (u32)lane * 16;
      auto issue = [&](int s) {
        int kt = s + rot2; kt = kt >= 28 ? kt - 28 : kt;
        glds8<4096, true>(hb + (size_t)kt * 16384, vo, vo + 2048, (u32)((s & 3) * 16384));
      };
      bar();
      issue(0); issue(1); issue(2); issue(3);
      waitv<24>();
      bar();
#pragma unroll 1
      for (int s = 0; s < 24; s++) { waitv<16>(); bar(); issue(s + 4); }
      waitv<16>(); bar(); waitv<8>(); bar(); waitv<0>(); bar();
    } else if (w == 5) {
      const int row2 = lane >> 3;
      const u32 vo2 = (u32)(row2 * 1024 + (((lane & 7) ^ ((row2 & 2) << 1)) << 4));
      const char* w2b = (const char*)w2 + bn2 * 128;
      auto issue2 = [&](int s) {
        int kt = s + rot2; kt = kt >= 28 ? kt - 28 : kt;
        glds8<16384, false>(w2b + (size_t)kt * 65536, vo2, vo2 + 8192, (u32)((s & 3) * 16384 + 8192));
      };
      if (isg1) {
        const int rot1 = 2 * ((2 * bm + bn) & 7);
        const int k = lane >> 4;
        const u32 vo = (u32)(k * 3584 + (((lane & 15) ^ (((k & 1) << 1) | ((k & 2) << 2))) << 4));
        const char* wb = (const char*)w1 + bn * 256;
        auto issue = [&](int s) { glds8<28672, false>(wb + (size_t)((s + rot1) & 15) * 114688, vo, vo + 14336, (u32)((s & 3) * 16384 + 8192)); };
        issue(0); issue(1); issue(2); issue(3);
        waitv<24>();
        bar();
#pragma unroll 1
        for (int s = 0; s < 12; s++) { waitv<16>(); bar(); issue(s + 4); }
#pragma unroll 1
        for (int s = 12; s < 16; s++) { waitv<16>(); bar(); issue2(s - 12); }
      } else {
        bar();
        issue2(0); issue2(1); issue2(2); issue2(3);
      }
      bar();
      waitv<24>();
      bar();
#pragma unroll 1
      for (int s = 0; s < 24; s++) { waitv<16>(); bar(); issue2(s + 4); }
      waitv<16>(); bar(); waitv<8>(); bar(); waitv<0>(); bar();
    } else if (w == 6) {
      Shake S;
      if (isg1) {
        shake_init(S, ws, (u32)gb);
        if (lane < 4) ldsst<u32>(LA_ZERO + lane * 4, 0u);
        bool rel = shake_step(S, ws, (u32)gb, lane);
#pragma unroll 1
        for (int it = 0; it < 12; it++) {
          bar();
          if (!rel) rel = shake_step(S, ws, (u32)gb, lane);
        }
        STAMP(1);
        if (!rel) shake_finish(S, ws, (u32)gb, lane);
        waitv<0>();
        STAMP(2);
        u32x4 gs, gc;
        u32 bv;
        const char* sb_ = ws + WS_STAT + bm * 1024;
        const char* cb_ = ws + WS_CS + bn * 1024;
        asm volatile("global_load_dwordx4 %0, %3, %5 sc1\n\tglobal_load_dwordx4 %1, %3, %6 sc1\n\tglobal_load_dword %2, %4, %7"
                     : "=&v"(gs), "=&v"(gc), "=&v"(bv) : "v"((u32)lane * 16), "v"((u32)lane * 4), "s"(sb_), "s"(cb_), "s"(b1 + bn * 128) : "memory");
        bar(); bar(); bar(); bar();
        asm volatile("s_waitcnt vmcnt(0)" : "+v"(gs), "+v"(gc), "+v"(bv)::"memory");
        u32 spins = 0;
        while (!__all(gs[1] == EP && gs[3] == EP) && ++spins < SPIN_MAX) gs = ld16c_wait(sb_, (u32)lane * 16);
        while (!__all(gc[1] == EP && gc[3] == EP) && ++spins < SPIN_MAX) gc = ld16c_wait(cb_, (u32)lane * 16);
#pragma unroll
        for (int e = 0; e < 2; e++) {
          u32 p = gs[2 * e];
          u32 nm = (p & 0xffffu) ^ 0x8000u;
          ldsst<u32x4>(LA_XA + (2 * lane + e) * 16, (u32x4){nm | (nm << 16), p >> 16, 0u, 0u});
          ldsst<float>(LA_RS + (2 * lane + e) * 4, 1.f / bfhi(p));
          float c = __uint_as_float(gc[2 * e]);
          float chi = rbf(c);
          ldsst<u32x4>(LA_WA + (2 * lane + e) * 16, (u32x4){pkbf(chi, c - chi), e ? bv >> 16 : bv & 0xffffu, 0u, 0u});
        }
        STAMP(3);
        bar();
      } else {
        if (ci < 28) st4c(ws, WS_CS + (u32)(ci * 512 + lane * 8 + 4), 0u);
        shake_init(S, ws, (u32)gb);
        bar();
        shake_finish(S, ws, (u32)gb, lane);
      }
      {
        const char* xb2 = (const char*)x + (size_t)bm2 * 65536 + bn2 * 128;
        u32 vo = (u32)((lane >> 3) * 1024 + (((lane & 7) ^ (lane >> 3)) << 4));
        glds8<16384, false>(xb2, vo, vo + 8192, LA_OUT);
      }
      {
        u32 spins = 0;
        for (;;) {
          u32 f = ld4c_wait(ws + WS_HFLAG + bm2 * 128, (u32)(lane < 28 ? lane : 0) * 4);
          if (__all(f == EP) || ++spins > SPIN_MAX) break;
        }
      }
      STAMP(4);
      bar();
      const int ya_ = bm2 >> 2;
      const char* yfb = ws + WS_YFLAG + ((ya_ >> 2) * 512 + bn2 * 64) * 4;
      bool got = false;
      auto ytile = [&]() {
        const char* yb = ws + WS_Y + (size_t)bn2 * 64 * 4096 + ya_ * 512 + (bm2 & 3) * 64;
        u32 vo = (u32)((lane >> 3) * 4096 + ((lane >> 2) & 1) * 256 + (lane & 3) * 16);
        glds8<65536, true>(yb, vo, vo + 32768, LA_YT);
      };
      asm volatile("v_mov_b32 v125, 0" ::: "v125");
#pragma unroll 1
      for (int it = 0; it < 28; it++) {
        if (!got) {
          u64 ne;
          asm volatile("v_cmp_ne_u32 %0, 1, v125" : "=s"(ne)::"memory");
          if (ne == 0) { got = true; ytile(); STAMP(6); }
          else if ((it & 3) == 0) asm volatile("global_load_dword v125, %0, %1 sc1" ::"v"((u32)lane * 4), "s"(yfb) : "memory", "v125");
        }
        bar();
      }
      if (!got) {
        u32 spins = 0, yf;
        waitv<0>();
        do { yf = ld4c_wait(yfb, (u32)lane * 4); } while (!__all(yf == EP) && ++spins < SPIN_MAX);
        ytile();
      }
      waitv<0>();
      STAMP(5);
      bar();
    }
  } else {
    const int G = w >> 2, kch = (w >> 1) & 1, kmh = w & 1;
    const int dch = 2 * tb + kch;
    u32x4 xr;
    u32 pa, pb, pc_, pd, pe, pf, pl, pk_;
    {
      u32 vo = (u32)((8 * tb + w) * 1024 + lane * 16);
      u32 po = (u32)((dch * 128 + kmh * 64 + lane) * 2);
      u32 lo = (u32)(dch * 2);
      asm volatile(
          "global_load_dwordx4 %0, %9, %12\n\t"
          "global_load_ushort %1, %10, %13\n\t"
          "global_load_ushort %2, %10, %14\n\t"
          "global_load_ushort %3, %10, %15\n\t"
          "global_load_ushort %4, %10, %16\n\t"
          "global_load_ushort %5, %10, %17\n\t"
          "global_load_ushort %6, %10, %18\n\t"
          "global_load_ushort %7, %11, %19\n\t"
          "global_load_ushort %8, %11, %20"
          : "=&v"(xr), "=&v"(pa), "=&v"(pb), "=&v"(pc_), "=&v"(pd), "=&v"(pe), "=&v"(pf), "=&v"(pl), "=&v"(pk_)
          : "v"(vo), "v"(po), "v"(lo), "s"(x), "s"(Are), "s"(Aim), "s"(Bre), "s"(Bim), "s"(Cre), "s"(Cim), "s"(ldt), "s"(Dsk)
          : "memory");
    }
    Shake S;
    bool rel = false;
    if (w == 7) {
      u32 off;
      if (lane < 8) off = WS_STAT + (u32)(8 * tb + lane) * 8 + 4;
      else if (lane < 12) off = WS_YFLAG + (u32)((lane & 1) * 512 + 2 * tb + ((lane >> 1) & 1)) * 4;
      else if (lane < 16 && isg1) off = WS_HFLAG + (u32)((bm * 2 + ((lane >> 1) & 1)) * 32 + bn * 2 + (lane & 1)) * 4;
      else off = WS_UFLAG + (u32)tb * 4;
      if (lane < 17) st4c(ws, off, 0u);
      shake_init(S, ws, (u32)gb);
      rel = shake_step(S, ws, (u32)gb, lane);
      waitv<3>();
    } else {
      waitv<0>();
    }
    asm volatile("" : "+v"(xr), "+v"(pa), "+v"(pb), "+v"(pc_), "+v"(pd), "+v"(pe), "+v"(pf), "+v"(pl), "+v"(pk_)::"memory");
    STAMP(1);
    {
      float v[8];
#pragma unroll
      for (int j = 0; j < 4; j++) { v[2 * j] = bf2f(xr[j]); v[2 * j + 1] = bfhi(xr[j]); }
      float mean = wave_sum(((v[0] + v[1]) + (v[2] + v[3])) + ((v[4] + v[5]) + (v[6] + v[7]))) * (1.f / 512.f);
      float q = 0.f;
#pragma unroll
      for (int j = 0; j < 8; j++) { float d = v[j] - mean; q = __builtin_fmaf(d, d, q); }
      float var = wave_sum(q) * (1.f / 512.f);
      float mb = rbf(mean), sd = rbf(__builtin_sqrtf(rbf(rbf(var) + 1e-5f)));
      float rs = 1.f / sd;
      u32x4 o;
#pragma unroll
      for (int j = 0; j < 4; j++) {
        u32 dd = pkbf(v[2 * j] - mb, v[2 * j + 1] - mb);
        float d0 = bf2f(dd), d1 = bfhi(dd);
        float q0 = d0 * rs, q1 = d1 * rs;
        q0 = __builtin_fmaf(__builtin_fmaf(-q0, sd, d0), rs, q0);
        q1 = __builtin_fmaf(__builtin_fmaf(-q1, sd, d1), rs, q1);
        u32 qq = pkbf(q0, q1);
        o[j] = pkh(bf2f(qq), bfhi(qq));
      }
      ldsst<u32x4>(LB_LNS + w * 1088 + lane * 16, o);
      if (lane == 0) ldsst<u32>(LB_ST + w * 4, (__float_as_uint(mb) >> 16) | (__float_as_uint(sd) & 0xffff0000u));
    }
    bar();
    if (w == 7) {
      const int li = lane & 15;
      u32 a = LB_LNS + (li >> 2) * 1088 + ((lane & 48) + 4 * (li & 3)) * 2;
#pragma unroll
      for (int j = 0; j < 8; j++) {
        u32x2 p0 = ldstr(a + j * 128), p1 = ldstr(a + j * 128 + 4 * 1088);
        st16c(ws + WS_U + (size_t)tb * 8192, (u32)(j * 1024 + lane * 16), (u32x4){p0[0], p0[1], p1[0], p1[1]});
      }
      u32 pay = ldsld<u32>(LB_ST + (lane & 7) * 4);
      if (lane < 8) st8c(ws, WS_STAT + (u32)(8 * tb + lane) * 8, (u32x2){pay, EP});
      if (!rel) rel = shake_step(S, ws, (u32)gb, lane);
    }
    STAMP(2);
    const int rr = lane & 15, kg = lane >> 4;
    {
      float dt = rbf(expf(bf2f(pl)));
      float a2 = (dt * bf2f(pa)) * 1.44269504088896341f;
      float bb = dt * bf2f(pb);
      f32x2 W = cmul((f32x2){bf2f(pe), bf2f(pf)}, (f32x2){dt * bf2f(pc_), dt * bf2f(pd)});
      const float C_HI = 0.15915494309189535f, C_LO = (float)(0.15915494309189533577 - (double)0.15915494309189535f);
      float p = bb * C_HI;
      float er = __builtin_fmaf(bb, C_HI, -p);
      float fhi = __uint_as_float(__float_as_uint(p) & 0xfffff000u);
      float flo = (p - fhi) + __builtin_fmaf(bb, C_LO, er);
      float g = 0.f;
      if (__any(a2 > 0.f)) { g = wave_max0(fmaxf(a2, 0.f)); a2 -= g; }
      float wsum = wave_sum(fabsf(W[0]) + fabsf(W[1]));
      float sig, isig;
      pow2scale(wsum, 13, sig, isig);
      const f32x2 W0 = {W[0] * sig, -W[1] * sig};
      const u32 T = LB_TAB + w * 8192;
      const u32 wbase = T + (lane >> 2) * 16 + (lane & 3) * 4;
      const u32 ra = T + rr * 256 + ((kg ^ rr) << 4);
      const u32 P = T + 4096;
      const float cs = G ? 8.f : 2.f, rs = G ? 128.f : 32.f, rb = G ? 512.f : 0.f;
      f32x2 zc = cpow(a2, -fhi, -flo, cs), zc4 = cpow(a2, -fhi, -flo, 4.f * cs);
      f32x2 zr = cpow(a2, fhi, flo, rs), zr4 = cpow(a2, fhi, flo, 4.f * rs);
      f32x2 zb = cpow(a2, fhi, flo, rb), zx = cpow(a2, fhi, flo, 0.5f * cs);
      f32x2 zc2 = cmul(zc, zc), zr2 = cmul(zr, zr);
      f32x2 c[4], r[4], e0[4], e1[4];
      c[0] = W0; c[1] = cmul(W0, zc); r[0] = zb; r[1] = cmul(zb, zr);
      c[2] = cmul(c[0], zc2); c[3] = cmul(c[1], zc2); r[2] = cmul(r[0], zr2); r[3] = cmul(r[1], zr2);
      if (w == 7 && !rel) rel = shake_step(S, ws, (u32)gb, lane);
#pragma unroll
      for (int o = 0; o < 4; o++) {
        if (o > 0) { cmul4(c[0], c[1], c[2], c[3], zc4); cmul4(r[0], r[1], r[2], r[3], zr4); }
        if (o == 0) { e0[0] = r[0]; e0[1] = r[1]; e0[2] = r[2]; e0[3] = r[3]; cmul4(e0[0], e0[1], e0[2], e0[3], zx); }
        if (o == 1) { e1[0] = r[0]; e1[1] = r[1]; e1[2] = r[2]; e1[3] = r[3]; cmul4(e1[0], e1[1], e1[2], e1[3], zx); }
#pragma unroll
        for (int j = 0; j < 4; j++) {
          int e = 4 * o + j;
          u32 a = wbase ^ (u32)(e << 4);
          ldsst<u32>(a + e * 256, pkh(r[j][0], r[j][1]));
          ldsst<u32>(a + e * 256 + 4096, pkh(c[j][0], c[j][1]));
        }
      }
      f16x8 af[4], bf[4];
#pragma unroll
      for (int s = 0; s < 4; s++) { af[s] = ldsld<f16x8>(ra ^ (u32)(s << 6)); bf[s] = ldsld<f16x8>((ra ^ (u32)(s << 6)) + 4096); }
#pragma unroll
      for (int j = 0; j < 4; j++) {
        ldsst<u32>((wbase ^ (u32)(j << 4)) + j * 256, pkh(e0[j][0], e0[j][1]));
        ldsst<u32>((wbase ^ (u32)((4 + j) << 4)) + (4 + j) * 256, pkh(e1[j][0], e1[j][1]));
      }
      f16x8 ag[4];
#pragma unroll
      for (int s = 0; s < 4; s++) ag[s] = ldsld<f16x8>(ra ^ (u32)(s << 6));
      f32x4 d0 = {0.f, 0.f, 0.f, 0.f}, d1 = {0.f, 0.f, 0.f, 0.f};
#pragma unroll
      for (int s = 0; s < 4; s++) d0 = __builtin_amdgcn_mfma_f32_16x16x32_f16(af[s], bf[s], d0, 0, 0, 0);
#pragma unroll
      for (int s = 0; s < 4; s++) d1 = __builtin_amdgcn_mfma_f32_16x16x32_f16(ag[s], bf[s], d1, 0, 0, 0);
      const int thr = G ? 1 : 2;
      const bool fine = kg < thr;
      u32 i0 = fine ? (u32)((G ? 384 : 0) + 128 * kg + 2 * rr) : (u32)((G ? 512 : 256) + 64 * (kg - thr) + rr);
      u32 st = fine ? 128u : 64u;
      u32 a0 = P + i0 * 4, a1 = fine ? a0 + 4 : P + 704 * 4;
      u32 st1 = fine ? 128u : 0u;
      if (g > 0.f) {
#pragma unroll
        for (int e = 0; e < 4; e++) {
          float l0 = rb + rs * (float)(4 * kg + e) + cs * (float)rr;
          d0[e] *= __builtin_amdgcn_exp2f(g * l0);
          d1[e] *= __builtin_amdgcn_exp2f(g * (l0 + 0.5f * cs));
        }
      }
#pragma unroll
      for (int e = 0; e < 4; e++) {
        ldsst<float>(a0 + e * st, d0[e] * isig);
        ldsst<float>(a1 + e * st1, d1[e] * isig);
      }
      if (lane == 0) { ldsst<float>(P + 764 * 4, wsum * __builtin_amdgcn_exp2f(g * 2048.f)); ldsst<float>(P + 760 * 4, 0.f); }
    }
    if (w == 7) {
      waitv<0>();
      if (lane == 0) st4c(ws, WS_UFLAG + (u32)tb * 4, EP);
      if (!rel) rel = shake_step(S, ws, (u32)gb, lane);
    }
    STAMP(3);
    bar();
    STAMP(4);
    float kinv;
    u32 PK[5], QK[5];
    const int ech = tid >> 8, eg = tid & 255;
    {
      const u32 pb0 = LB_TAB + 4096 + (u32)ech * 16384;
      float bnd = ldsld<float>(pb0 + 764 * 4) + ldsld<float>(pb0 + 8192 + 764 * 4);
      float ks, ki;
      pow2scale(bnd, 14, ks, ki);
      float bnd2 = ldsld<float>(LB_TAB + 4096 + kch * 16384 + 764 * 4) + ldsld<float>(LB_TAB + 4096 + kch * 16384 + 8192 + 764 * 4);
      float ks2;
      pow2scale(bnd2, 14, ks2, kinv);
      float v[11];
#pragma unroll
      for (int e = 0; e < 11; e++) {
        int lag = 2047 - 8 * eg - e;
        u32 u = __float_as_uint((float)lag);
        u32 id = ((u + 0x7fffu + ((u >> 16) & 1u)) >> 16) - 0x4300u + 128u;
        id = lag < 128 ? (u32)(lag < 0 ? 760 : lag) : id;
        u32 a = pb0 + id * 4 + ((id >= 384u && lag >= 0) ? 32768u : 0u);
        v[e] = (ldsld<float>(a) + ldsld<float>(a + 8192)) * ks;
      }
#pragma unroll
      for (int j = 0; j < 5; j++) { PK[j] = pkh(v[2 * j], v[2 * j + 1]); QK[j] = pkh(v[2 * j + 1], v[2 * j + 2]); }
    }
    bar();
    {
      u32 a = LB_KCP + ech * 16640 + eg * 16;
      ldsst<u32x4>(a, (u32x4){PK[0], PK[1], PK[2], PK[3]});
      ldsst<u32x4>(a + 4160, (u32x4){QK[0], QK[1], QK[2], QK[3]});
      ldsst<u32x4>(a + 8320, (u32x4){PK[1], PK[2], PK[3], PK[4]});
      ldsst<u32x4>(a + 12480, (u32x4){QK[1], QK[2], QK[3], QK[4]});
      if (tid < 16) ldsst<u32x4>(LB_KCP + (tid >> 1) * 4160 + (256 + (tid & 1)) * 16, (u32x4){0, 0, 0, 0});
      if (tid < 64) ldsst<u32x4>(LB_UL + (tid >> 5) * 4608 + (tid & 31) * 16, (u32x4){0, 0, 0, 0});
    }
    STAMP(5);
    if (w == 7) {
      if (!rel) shake_finish(S, ws, (u32)gb, lane);
      waitv<0>();
      STAMP(7);
      u32 spins = 0;
      for (;;) {
        u32x4 uf = ld16c_wait(ws + WS_UFLAG, (u32)lane * 16);
        if (__all(uf[0] == EP && uf[1] == EP && uf[2] == EP && uf[3] == EP) || ++spins > SPIN_MAX) break;
      }
      STAMP(8);
      u32 vo = (u32)lane * 8192;
#pragma unroll
      for (int j = 0; j < 4; j++)
        glds2<4608, true>(ws + WS_U + (size_t)tb * 32 + (size_t)j * 524288, vo, vo + 16, LB_UL + 512 + (u32)j * 1024);
      waitv<0>();
    }
    bar();
    STAMP(6);
    if (w < 4) {
      const int n = rr;
      f32x4 acc[8];
#pragma unroll
      for (int a = 0; a < 8; a++) acc[a] = (f32x4){0.f, 0.f, 0.f, 0.f};
      const int cc = (3 - n) & 3;
      u32 ub = LB_UL + kch * 4608 + (u32)((256 + 16 * (n - 8 * kmh) - 16 + 8 * kg) * 2);
      u32 ab = LB_KCP + kch * 16640 + cc * 4160 + (u32)((2031 - n + 8 * kg - cc - 1792 - 128 * kmh) * 2);
      f16x8 Bq[8];
      u32x2 Al[4], Ah[4];
#pragma unroll
      for (int q = 0; q < 8; q++) Bq[q] = ldsld<f16x8>(ub + q * 512);
#pragma unroll
      for (int i = 0; i < 4; i++) { Al[i] = ldsldv<u32x2>(ab + (7 - i) * 512); Ah[i] = ldsldv<u32x2>(ab + (7 - i) * 512 + 8); }
#pragma unroll 1
      for (int it = 0; it < 4; it++) {
#pragma unroll
        for (int i = 0; i < 8; i++) {
          const int in = (i + 4) & 7;
          u32 a = ab + (7 - in) * 512 - (i >= 4 ? 64 : 0);
          f16x8 A = __builtin_bit_cast(f16x8, (u32x4){Al[i & 3][0], Al[i & 3][1], Ah[i & 3][0], Ah[i & 3][1]});
          acc[7] = __builtin_amdgcn_mfma_f32_16x16x32_f16(A, Bq[7 - i], acc[7], 0, 0, 0);
#pragma unroll
          for (int a2 = 6; a2 >= i; a2--) acc[a2] = __builtin_amdgcn_mfma_f32_16x16x32_f16(A, Bq[a2 - i], acc[a2], 0, 0, 0);
          __builtin_amdgcn_sched_barrier(0);
          Al[i & 3] = ldsldv<u32x2>(a); Ah[i & 3] = ldsldv<u32x2>(a + 8);
          Bq[7 - i] = ldsld<f16x8>(ub - 64 + (7 - i) * 512);
          __builtin_amdgcn_sched_barrier(0);
        }
        ub -= 64; ab -= 64;
      }
      STAMP(7);
      if (kmh) {
#pragma unroll
        for (int a = 0; a < 4; a++) { f32x4 t = acc[a]; acc[a] = acc[4 + a]; acc[4 + a] = t; asm volatile("" : "+v"(acc[a]), "+v"(acc[4 + a])); }
      }
#pragma unroll
      for (int a = 0; a < 4; a++) ldsst<f32x4>(LB_RED + (u32)(w * 4096 + a * 1024 + lane * 16), acc[4 + a]);
      bar();
      const float dsk = bf2f(pk_);
      char* yp = ws + WS_Y + (size_t)dch * 4096 + kmh * 2048;
#pragma unroll
      for (int pr = 0; pr < 2; pr++) {
        u32 pk[2][2];
#pragma unroll
        for (int j = 0; j < 2; j++) {
          const int a = 2 * pr + j;
          f32x4 yy = acc[a] + ldsld<f32x4>(LB_RED + (u32)((w ^ 1) * 4096 + a * 1024 + lane * 16));
          f16x4 uu = ldsld<f16x4>(LB_UL + kch * 4608 + 512 + (u32)((256 * (4 * kmh + a) + 16 * n + 4 * kg) * 2));
          u32 y01 = pkbf(yy[0] * kinv, yy[1] * kinv), y23 = pkbf(yy[2] * kinv, yy[3] * kinv);
          u32 s01 = pkbf((float)uu[0] * dsk, (float)uu[1] * dsk), s23 = pkbf((float)uu[2] * dsk, (float)uu[3] * dsk);
          pk[j][0] = pkbf(bf2f(y01) + bf2f(s01), bfhi(y01) + bfhi(s01));
          pk[j][1] = pkbf(bf2f(y23) + bf2f(s23), bfhi(y23) + bfhi(s23));
        }
        auto s0 = __builtin_amdgcn_permlane16_swap(pk[0][0], pk[1][0], false, false);
        auto s1 = __builtin_amdgcn_permlane16_swap(pk[0][1], pk[1][1], false, false);
        st16c(yp, (u32)((2 * pr + (kg & 1)) * 512 + (kg >> 1) * 256 + n * 16), (u32x4){s0[0], s1[0], s0[1], s1[1]});
      }
      waitv<0>();
      if (lane == 0) st4c(ws, WS_YFLAG + (u32)(kmh * 512 + dch) * 4, EP);
      STAMP(8);
    }
  }
  STAMP_OUT();
}

extern "C" void kernel_launch(void* const* d_in, const int* in_sizes, int n_in, void* d_out, int out_size, void* d_ws,
                              size_t ws_size, hipStream_t stream) {
  fused_kernel<<<512, 512, 0, stream>>>((const u16*)d_in[0], (const u16*)d_in[1], (const u16*)d_in[2], (const u16*)d_in[3],
                                        (const u16*)d_in[4], (const u16*)d_in[5], (const u16*)d_in[6], (const u16*)d_in[7],
                                        (const u16*)d_in[8], (const u16*)d_in[10], (const u16*)d_in[11], (const u16*)d_in[12],
                                        (u16*)d_out, (char*)d_ws);
}
```

```cpp
#include <hip/hip_runtime.h>
#include <stdint.h>
#include <type_traits>

typedef unsigned short u16;
typedef unsigned int u32;
typedef unsigned long long u64;
typedef __attribute__((ext_vector_type(8))) __bf16 bf16x8;
typedef __attribute__((ext_vector_type(2))) __bf16 bf16x2;
typedef __attribute__((ext_vector_type(8))) _Float16 f16x8;
typedef __attribute__((ext_vector_type(4))) _Float16 f16x4;
typedef __attribute__((ext_vector_type(2))) _Float16 f16x2;
typedef __attribute__((ext_vector_type(2))) float f32x2;
typedef __attribute__((ext_vector_type(4))) float f32x4;
typedef __attribute__((ext_vector_type(16))) float f32x16;
typedef __attribute__((ext_vector_type(4))) u32 u32x4;
typedef __attribute__((ext_vector_type(2))) u32 u32x2;
typedef __attribute__((ext_vector_type(4))) short s16x4;
#define AS3 __attribute__((address_space(3)))
#define DEVI __device__ __forceinline__
template <int N> using IC = std::integral_constant<int, N>;

constexpr u32 WS_STAT = 0;
constexpr u32 WS_CS = 16384;
constexpr u32 WS_UFLAG = 30720;
constexpr u32 WS_YFLAG = 31744;
constexpr u32 WS_HFLAG = 35840;
constexpr u32 WS_R = 40960;
constexpr u32 WS_A = 45056;
constexpr u32 WS_N = 47104;
constexpr size_t WS_U = 1u << 20, WS_Y = 4u << 20, WS_H = 8u << 20, WS_DBG = 32u << 20;
constexpr u32 LB = 98304;
constexpr u32 L_TAB = LB;
constexpr u32 L_KCP = LB;
constexpr u32 L_YT = LB, L_OUT = LB + 8192;
constexpr u32 L_UL = LB + 33280;
constexpr u32 L_RED = LB + 42496;
constexpr u32 L_XA = LB + 58880, L_WA = LB + 59904, L_RS = LB + 60928, L_ZERO = LB + 64512;
constexpr u32 L_SYNC = LB + 64576;
constexpr u32 L_LNS = 81920;
constexpr u32 Y_BST = L_SYNC;
constexpr u32 Y_HLP = L_SYNC + 16;
constexpr u32 Y_UOK = L_SYNC + 20;
constexpr u32 Y_REL = L_SYNC + 24;
constexpr u32 Y_MD = L_SYNC + 32;
constexpr u32 Y_EXP = L_SYNC + 48;
constexpr u32 LSPIN_MAX = 1u << 20;
constexpr u32 SPIN_MAX = 1u << 15;
constexpr u32 EP = 1u;
constexpr int LEADER = 248;

template <typename T> DEVI T ldsld(u32 a) { return *(AS3 T*)(uintptr_t)a; }
template <typename T> DEVI T ldsldv(u32 a) { return *(volatile AS3 T*)(uintptr_t)a; }
template <typename T> DEVI void ldsst(u32 a, T v) { *(AS3 T*)(uintptr_t)a = v; }
DEVI u32x2 ldstr(u32 a) { return __builtin_bit_cast(u32x2, __builtin_amdgcn_ds_read_tr16_b64_v4i16((AS3 s16x4*)(uintptr_t)a)); }
DEVI float bf2f(u32 v) { return __uint_as_float(v << 16); }
DEVI float bfhi(u32 v) { return __uint_as_float(v & 0xffff0000u); }
DEVI float rbf(float f) { return (float)(__bf16)f; }
DEVI u32 pkbf(float a, float b) { bf16x2 v = {(__bf16)a, (__bf16)b}; return __builtin_bit_cast(u32, v); }
DEVI u32 pkh(float a, float b) { f16x2 v = {(_Float16)a, (_Float16)b}; return __builtin_bit_cast(u32, v); }

template <int STEP, bool COH>
DEVI void glds2(const void* sbase, u32 v0, u32 v1, u32 lds0) {
  u32 keep;
  if (COH)
    asm volatile("s_mov_b32 %0, m0\n\ts_mov_b32 m0, %4\n\ts_nop 0\n\tglobal_load_lds_dwordx4 %1, %3 sc1\n\t"
                 "s_add_u32 m0, m0, %5\n\ts_nop 0\n\tglobal_load_lds_dwordx4 %2, %3 sc1\n\ts_mov_b32 m0, %0"
                 : "=&s"(keep) : "v"(v0), "v"(v1), "s"(sbase), "s"(lds0), "n"(STEP) : "memory", "scc");
  else
    asm volatile("s_mov_b32 %0, m0\n\ts_mov_b32 m0, %4\n\ts_nop 0\n\tglobal_load_lds_dwordx4 %1, %3\n\t"
                 "s_add_u32 m0, m0, %5\n\ts_nop 0\n\tglobal_load_lds_dwordx4 %2, %3\n\ts_mov_b32 m0, %0"
                 : "=&s"(keep) : "v"(v0), "v"(v1), "s"(sbase), "s"(lds0), "n"(STEP) : "memory", "scc");
}
template <int SSTEP, bool COH>
DEVI void glds8(const char* sbase, u32 v0, u32 v1, u32 lds0) {
#pragma unroll
  for (int j = 0; j < 4; j++) glds2<1024, COH>(sbase + (size_t)j * SSTEP, v0, v1, lds0 + j * 2048);
}
DEVI u32x4 mksrd(const void* p, u32 bytes) {
  u64 a = (u64)p;
  return (u32x4){(u32)a, (u32)(a >> 32) & 0xffffu, bytes, 0x00020000u};
}
template <bool COHA>
DEVI void dma4(u32x4 sA, u32 soA, u32 vA0, u32 vA1, u32x4 sB, u32 soB, u32 vB0, u32 vB1, u32 ldsA) {
  if (COHA)
    asm volatile("s_mov_b32 m0, %8\n\ts_nop 0\n\tbuffer_load_dwordx4 %2, %0, %1 offen sc1 lds\n\t"
                 "buffer_load_dwordx4 %3, %0, %1 offen offset:1024 sc1 lds\n\ts_add_u32 m0, m0, 0x2000\n\ts_nop 0\n\t"
                 "buffer_load_dwordx4 %6, %4, %5 offen lds\n\tbuffer_load_dwordx4 %7, %4, %5 offen offset:1024 lds"
                 ::"s"(sA), "s"(soA), "v"(vA0), "v"(vA1), "s"(sB), "s"(soB), "v"(vB0), "v"(vB1), "s"(ldsA) : "memory", "scc");
  else
    asm volatile("s_mov_b32 m0, %8\n\ts_nop 0\n\tbuffer_load_dwordx4 %2, %0, %1 offen lds\n\t"
                 "buffer_load_dwordx4 %3, %0, %1 offen offset:1024 lds\n\ts_add_u32 m0, m0, 0x2000\n\ts_nop 0\n\t"
                 "buffer_load_dwordx4 %6, %4, %5 offen lds\n\tbuffer_load_dwordx4 %7, %4, %5 offen offset:1024 lds"
                 ::"s"(sA), "s"(soA), "v"(vA0), "v"(vA1), "s"(sB), "s"(soB), "v"(vB0), "v"(vB1), "s"(ldsA) : "memory", "scc");
}
template <bool COH>
DEVI void dma2(u32x4 sA, u32 soA, u32 vA0, u32 vA1, u32 ldsA) {
  if (COH)
    asm volatile("s_mov_b32 m0, %4\n\ts_nop 0\n\tbuffer_load_dwordx4 %2, %0, %1 offen sc1 lds\n\t"
                 "buffer_load_dwordx4 %3, %0, %1 offen offset:1024 sc1 lds" ::"s"(sA), "s"(soA), "v"(vA0), "v"(vA1), "s"(ldsA) : "memory");
  else
    asm volatile("s_mov_b32 m0, %4\n\ts_nop 0\n\tbuffer_load_dwordx4 %2, %0, %1 offen lds\n\t"
                 "buffer_load_dwordx4 %3, %0, %1 offen offset:1024 lds" ::"s"(sA), "s"(soA), "v"(vA0), "v"(vA1), "s"(ldsA) : "memory");
}
template <bool COHA>
DEVI void dma11(u32x4 sA, u32 soA, u32 vA, u32x4 sB, u32 soB, u32 vB, u32 ldsA) {
  if (COHA)
    asm volatile("s_mov_b32 m0, %6\n\ts_nop 0\n\tbuffer_load_dwordx4 %2, %0, %1 offen sc1 lds\n\ts_add_u32 m0, m0, 0x2000\n\ts_nop 0\n\t"
                 "buffer_load_dwordx4 %5, %3, %4 offen lds" ::"s"(sA), "s"(soA), "v"(vA), "s"(sB), "s"(soB), "v"(vB), "s"(ldsA) : "memory", "scc");
  else
    asm volatile("s_mov_b32 m0, %6\n\ts_nop 0\n\tbuffer_load_dwordx4 %2, %0, %1 offen lds\n\ts_add_u32 m0, m0, 0x2000\n\ts_nop 0\n\t"
                 "buffer_load_dwordx4 %5, %3, %4 offen lds" ::"s"(sA), "s"(soA), "v"(vA), "s"(sB), "s"(soB), "v"(vB), "s"(ldsA) : "memory", "scc");
}
template <bool COH>
DEVI void dma1(u32x4 sA, u32 soA, u32 vA, u32 ldsA) {
  if (COH) asm volatile("s_mov_b32 m0, %3\n\ts_nop 0\n\tbuffer_load_dwordx4 %2, %0, %1 offen sc1 lds" ::"s"(sA), "s"(soA), "v"(vA), "s"(ldsA) : "memory");
  else asm volatile("s_mov_b32 m0, %3\n\ts_nop 0\n\tbuffer_load_dwordx4 %2, %0, %1 offen lds" ::"s"(sA), "s"(soA), "v"(vA), "s"(ldsA) : "memory");
}
template <bool COHA>
DEVI void dma21(u32x4 sA, u32 soA, u32 vA0, u32 vA1, u32x4 sB, u32 soB, u32 vB, u32 ldsA, u32 ldsB) {
  if (COHA)
    asm volatile("s_mov_b32 m0, %7\n\ts_nop 0\n\tbuffer_load_dwordx4 %2, %0, %1 offen sc1 lds\n\t"
                 "buffer_load_dwordx4 %3, %0, %1 offen offset:1024 sc1 lds\n\ts_mov_b32 m0, %8\n\ts_nop 0\n\tbuffer_load_dwordx4 %6, %4, %5 offen lds"
                 ::"s"(sA), "s"(soA), "v"(vA0), "v"(vA1), "s"(sB), "s"(soB), "v"(vB), "s"(ldsA), "s"(ldsB) : "memory");
  else
    asm volatile("s_mov_b32 m0, %7\n\ts_nop 0\n\tbuffer_load_dwordx4 %2, %0, %1 offen lds\n\t"
                 "buffer_load_dwordx4 %3, %0, %1 offen offset:1024 lds\n\ts_mov_b32 m0, %8\n\ts_nop 0\n\tbuffer_load_dwordx4 %6, %4, %5 offen lds"
                 ::"s"(sA), "s"(soA), "v"(vA0), "v"(vA1), "s"(sB), "s"(soB), "v"(vB), "s"(ldsA), "s"(ldsB) : "memory");
}
DEVI void rawbar() { asm volatile("" ::: "memory"); __builtin_amdgcn_s_barrier(); asm volatile("" ::: "memory"); }
DEVI void st16c(void* sbase, u32 voff, u32x4 v) {
  asm volatile("global_store_dwordx4 %0, %1, %2 sc1\n\ts_nop 1" ::"v"(voff), "v"(v), "s"(sbase) : "memory");
}
DEVI void st16(void* sbase, u32 voff, u32x4 v) {
  asm volatile("global_store_dwordx4 %0, %1, %2\n\ts_nop 1" ::"v"(voff), "v"(v), "s"(sbase) : "memory");
}
DEVI void st8c(void* sbase, u32 voff, u32x2 v) {
  asm volatile("global_store_dwordx2 %0, %1, %2 sc1\n\ts_nop 1" ::"v"(voff), "v"(v), "s"(sbase) : "memory");
}
DEVI void st4c(void* sbase, u32 voff, u32 v) {
  asm volatile("global_store_dword %0, %1, %2 sc1\n\ts_nop 1" ::"v"(voff), "v"(v), "s"(sbase) : "memory");
}
DEVI u32 ld4c_wait(const void* sbase, u32 voff) {
  u32 r;
  asm volatile("global_load_dword %0, %1, %2 sc1\n\ts_waitcnt vmcnt(0)" : "=&v"(r) : "v"(voff), "s"(sbase) : "memory");
  return r;
}
DEVI u32x4 ld16c_wait(const void* sbase, u32 voff) {
  u32x4 r;
  asm volatile("global_load_dwordx4 %0, %1, %2 sc1\n\ts_waitcnt vmcnt(0)" : "=&v"(r) : "v"(voff), "s"(sbase) : "memory");
  return r;
}
template <int N> DEVI void waitv() { asm volatile("s_waitcnt vmcnt(%0)" ::"n"(N) : "memory"); }
DEVI void bar() {
  asm volatile("s_waitcnt lgkmcnt(0)" ::: "memory");
  __builtin_amdgcn_s_barrier();
  asm volatile("" ::: "memory");
}
template <int CTRL, int RM> DEVI float dppf(float old, float v) {
  return __int_as_float(__builtin_amdgcn_update_dpp(__float_as_int(old), __float_as_int(v), CTRL, RM, 0xf, false));
}
DEVI float wave_sum(float v) {
  v += dppf<0x111, 0xf>(0.f, v); v += dppf<0x112, 0xf>(0.f, v); v += dppf<0x114, 0xf>(0.f, v); v += dppf<0x118, 0xf>(0.f, v);
  v += dppf<0x142, 0xa>(0.f, v); v += dppf<0x143, 0xc>(0.f, v);
  return __int_as_float(__builtin_amdgcn_readlane(__float_as_int(v), 63));
}
DEVI float wave_max0(float v) {
  v = fmaxf(v, dppf<0x111, 0xf>(0.f, v)); v = fmaxf(v, dppf<0x112, 0xf>(0.f, v)); v = fmaxf(v, dppf<0x114, 0xf>(0.f, v));
  v = fmaxf(v, dppf<0x118, 0xf>(0.f, v)); v = fmaxf(v, dppf<0x142, 0xa>(0.f, v)); v = fmaxf(v, dppf<0x143, 0xc>(0.f, v));
  return __int_as_float(__builtin_amdgcn_readlane(__float_as_int(v), 63));
}
#define CM_MUL(t, a, b) "v_pk_mul_f32 " t ", " a ", " b " op_sel_hi:[0,1]\n\t"
#define CM_FMA(r, a, b, t) "v_pk_fma_f32 " r ", " a ", " b ", " t " op_sel:[1,1,0] op_sel_hi:[1,0,1] neg_lo:[1,0,0]\n\t"
DEVI f32x2 cmul(f32x2 a, f32x2 b) {
  f32x2 t, r;
  asm(CM_MUL("%0", "%1", "%2") : "=v"(t) : "v"(a), "v"(b));
  asm(CM_FMA("%0", "%1", "%2", "%3") : "=v"(r) : "v"(a), "v"(b), "v"(t));
  return r;
}
DEVI void cmul4(f32x2& a0, f32x2& a1, f32x2& a2, f32x2& a3, f32x2 z) {
  f32x2 t0, t1, t2, t3;
  asm(CM_MUL("%0", "%4", "%8") CM_MUL("%1", "%5", "%8") CM_MUL("%2", "%6", "%8") CM_MUL("%3", "%7", "%8")
      CM_FMA("%0", "%4", "%8", "%0") CM_FMA("%1", "%5", "%8", "%1") CM_FMA("%2", "%6", "%8", "%2") CM_FMA("%3", "%7", "%8", "%3")
      : "=&v"(t0), "=&v"(t1), "=&v"(t2), "=&v"(t3) : "v"(a0), "v"(a1), "v"(a2), "v"(a3), "v"(z));
  a0 = t0; a1 = t1; a2 = t2; a3 = t3;
}
DEVI f32x2 cpow(float a2, float fhi, float flo, float R) {
  float m = __builtin_amdgcn_exp2f(a2 * R);
  float ph = __builtin_fmaf(flo, R, __builtin_amdgcn_fractf(fhi * R));
  return (f32x2){m * __builtin_amdgcn_cosf(ph), m * __builtin_amdgcn_sinf(ph)};
}
DEVI void pow2scale(float mx, int target, float& s, float& inv) {
  int ex = (int)((__float_as_uint(mx) >> 23) & 0xff);
  int se = 254 + target - ex;
  se = se < 2 ? 2 : (se > 252 ? 252 : se);
  s = __uint_as_float((u32)se << 23);
  inv = __uint_as_float((u32)(254 - se) << 23);
}

DEVI void sig(u32 a, u32 v) {
  asm volatile("s_mov_b64 exec, 1\n\tds_write_b32 %0, %1\n\ts_mov_b64 exec, -1" ::"v"(a), "v"(v) : "memory");
}
DEVI u32 min4(u32x4 f) { return __builtin_amdgcn_readfirstlane(min(min(f[0], f[1]), min(f[2], f[3]))); }
DEVI void wait4(u32 a, u32 v) {
  u32 spins = 0;
  while (min4(ldsldv<u32x4>(a)) < v && ++spins < LSPIN_MAX) __builtin_amdgcn_s_sleep(1);
  asm volatile("" ::: "memory");
}
DEVI u32 min2(u32x2 f) { return __builtin_amdgcn_readfirstlane(min(f[0], f[1])); }
DEVI void wait2(u32 a, u32 v) {
  u32 spins = 0;
  while (min2(ldsldv<u32x2>(a)) < v && ++spins < LSPIN_MAX) {}
  asm volatile("" ::: "memory");
}
DEVI void wait4f(u32 a, u32 v) {
  u32 spins = 0;
  while (min4(ldsldv<u32x4>(a)) < v && ++spins < LSPIN_MAX) {}
  asm volatile("" ::: "memory");
}
DEVI void wait8(u32 a, u32 v) {
  u32 spins = 0;
  for (;;) {
    u32x4 f0 = ldsldv<u32x4>(a), f1 = ldsldv<u32x4>(a + 16);
    if (min(min4(f0), min4(f1)) >= v || ++spins > LSPIN_MAX) break;
  }
  asm volatile("" ::: "memory");
}
DEVI void wait1(u32 a, u32 v) {
  u32 spins = 0;
  while (__builtin_amdgcn_readfirstlane(ldsldv<u32>(a)) < v && ++spins < LSPIN_MAX) __builtin_amdgcn_s_sleep(1);
  asm volatile("" ::: "memory");
}

__global__ __launch_bounds__(768, 3) void fused_kernel(
    const u16* __restrict__ x, const u16* __restrict__ Are, const u16* __restrict__ Aim, const u16* __restrict__ Bre,
    const u16* __restrict__ Bim, const u16* __restrict__ Cre, const u16* __restrict__ Cim, const u16* __restrict__ ldt,
    const u16* __restrict__ Dsk, const u16* __restrict__ w1, const u16* __restrict__ b1, const u16* __restrict__ w2,
    u16* __restrict__ out, char* __restrict__ ws) {
  __shared__ __attribute__((aligned(16))) char sm[163840];
  asm volatile("" ::"v"(sm) : "memory");
  const int tid = threadIdx.x, lane = tid & 63;
  const int w = __builtin_amdgcn_readfirstlane(tid >> 6);
  const int b = blockIdx.x, xcd = b & 7, bi = b >> 3;
  const bool isg1 = bi < 28;
  const int bm = 2 * xcd + (bi & 1), bn = bi >> 1;
  const int tb = 32 * xcd + bi;
  const int bm2 = 4 * xcd + (bi & 3), bn2 = bi >> 2;
  const int ci = xcd * 4 + (bi - 28);
#define STAMP(i)
#define STAMP_OUT()
#define SB0 __builtin_amdgcn_sched_barrier(0)
  STAMP(0);
  if (w < 4) {
    const int hh = lane >> 5, blk = (lane >> 4) & 1, q4 = (lane & 15) >> 2, p4 = lane & 3;
    u32 sb = 0;
    const bool lead = b == LEADER && w == 1;
    u32 cand = 0;
#define LEAD_POLL() asm volatile("global_load_dwordx4 v[152:155], %0, %1 sc1\n\tglobal_load_dwordx4 v[156:159], %0, %1 offset:16 sc1" \
                                 ::"v"((u32)lane * 32), "s"(ws + WS_R) : "memory", "v152", "v153", "v154", "v155", "v156", "v157", "v158", "v159")
    {
      u32x4 xr0, xr1;
      asm volatile("global_load_dwordx4 %0, %2, %3\n\tglobal_load_dwordx4 %1, %2, %3 offset:1024"
                   : "=&v"(xr0), "=&v"(xr1) : "v"((u32)((8 * tb + 2 * w) * 1024 + lane * 16)), "s"(x) : "memory");
      if (lane < 2) st4c(ws, WS_STAT + (u32)(8 * tb + 2 * w + lane) * 8 + 4, 0u);
      if (lead) {
        LEAD_POLL();
        cand = __builtin_amdgcn_readfirstlane(ld4c_wait(ws, WS_N)) + 1u;
        for (;;) {
          u64 m0_, m1_, m2_, m3_;
          asm volatile("v_cmp_eq_u32 %0, %4, v153\n\tv_cmp_eq_u32 %1, %4, v155\n\tv_cmp_eq_u32 %2, %4, v157\n\tv_cmp_eq_u32 %3, %4, v159"
                       : "=&s"(m0_), "=&s"(m1_), "=&s"(m2_), "=&s"(m3_) : "s"(cand) : "memory");
          if ((m0_ | m1_ | m2_ | m3_) == 0) break;
          cand++;
        }
        if (lane == 0) st4c(ws, WS_N, cand);
        STAMP(10);
      }
      asm volatile("s_waitcnt vmcnt(1)" : "+v"(xr0), "+v"(xr1)::"memory");
      float v[2][8], mean[2], var[2];
      u32 pay[2];
#pragma unroll
      for (int r = 0; r < 2; r++) {
        u32x4 xr = r ? xr1 : xr0;
#pragma unroll
        for (int j = 0; j < 4; j++) { v[r][2 * j] = bf2f(xr[j]); v[r][2 * j + 1] = bfhi(xr[j]); }
        mean[r] = ((v[r][0] + v[r][1]) + (v[r][2] + v[r][3])) + ((v[r][4] + v[r][5]) + (v[r][6] + v[r][7]));
      }
      mean[0] = wave_sum(mean[0]) * (1.f / 512.f); mean[1] = wave_sum(mean[1]) * (1.f / 512.f);
#pragma unroll
      for (int r = 0; r < 2; r++) {
        float q = 0.f;
#pragma unroll
        for (int j = 0; j < 8; j++) { float d = v[r][j] - mean[r]; q = __builtin_fmaf(d, d, q); }
        var[r] = q;
      }
      var[0] = wave_sum(var[0]) * (1.f / 512.f); var[1] = wave_sum(var[1]) * (1.f / 512.f);
#pragma unroll
      for (int r = 0; r < 2; r++) {
        float mb = rbf(mean[r]), sd = rbf(__builtin_sqrtf(rbf(rbf(var[r]) + 1e-5f)));
        float rs = __builtin_amdgcn_rcpf(sd);
        u32x4 o;
#pragma unroll
        for (int j = 0; j < 4; j++) {
          u32 dd = pkbf(v[r][2 * j] - mb, v[r][2 * j + 1] - mb);
          float d0 = bf2f(dd), d1 = bfhi(dd);
          float q0 = d0 * rs, q1 = d1 * rs;
          q0 = __builtin_fmaf(__builtin_fmaf(-q0, sd, d0), rs, q0);
          q1 = __builtin_fmaf(__builtin_fmaf(-q1, sd, d1), rs, q1);
          u32 qq = pkbf(q0, q1);
          o[j] = pkh(bf2f(qq), bfhi(qq));
        }
        ldsst<u32x4>(L_LNS + (2 * w + r) * 1088 + lane * 16, o);
        pay[r] = (__float_as_uint(mb) >> 16) | (__float_as_uint(sd) & 0xffff0000u);
      }
      waitv<0>();
      bar();
      const int li = lane & 15;
      u32 a = L_LNS + (li >> 2) * 1088 + (w * 128 + (lane & 48) + 4 * (li & 3)) * 2;
#pragma unroll
      for (int j = 0; j < 2; j++) {
        u32x2 p0 = ldstr(a + j * 128), p1 = ldstr(a + j * 128 + 4 * 1088);
        st16c(ws + WS_U + (size_t)tb * 8192, (u32)(w * 2048 + j * 1024 + lane * 16), (u32x4){p0[0], p0[1], p1[0], p1[1]});
      }
      if (lane < 2) st8c(ws, WS_STAT + (u32)(8 * tb + 2 * w + lane) * 8, (u32x2){lane ? pay[1] : pay[0], EP});
    }
    __builtin_amdgcn_s_setprio(3);
    if (isg1) {
      const int wf = w & 1, wt = w >> 1;
      const int tl = wt * 64 + (lane & 31);
      const u32 xa0 = (u32)(tl * 64 + ((hh ^ ((tl >> 2) & 3)) << 4));
      const u32 wa0 = (u32)(8192 + (8 * hh + q4) * 256 +
                            (((8 * wf + 4 * (p4 & 1) + blk) ^ (((q4 & 1) << 1) | ((q4 & 2) << 2))) << 4) + 8 * (p4 >> 1));
      f32x16 acc[2][2];
#pragma unroll
      for (int i = 0; i < 16; i++) { acc[0][0][i] = 0.f; acc[0][1][i] = 0.f; acc[1][0][i] = 0.f; acc[1][1][i] = 0.f; }
      bf16x8 FX[2][2][2], FW[2][2][2];
      auto half = [&](auto P_, auto KS_) {
        constexpr int P = decltype(P_)::value, Q = 1 - P, ks = decltype(KS_)::value;
        const u32 ax = sb + (xa0 ^ (u32)(ks << 5)), aw0 = sb + wa0 + ks * 4096, aw1 = sb + (wa0 ^ 32u) + ks * 4096;
        u32x2 a0, a1, a2, a3;
        SB0; acc[0][0] = __builtin_amdgcn_mfma_f32_32x32x16_bf16(FW[P][ks][0], FX[P][ks][0], acc[0][0], 0, 0, 0); SB0;
        FX[Q][ks][0] = ldsld<bf16x8>(ax); a0 = ldstr(aw0);
        SB0; acc[0][1] = __builtin_amdgcn_mfma_f32_32x32x16_bf16(FW[P][ks][0], FX[P][ks][1], acc[0][1], 0, 0, 0); SB0;
        a1 = ldstr(aw0 + 1024); FX[Q][ks][1] = ldsld<bf16x8>(ax + 2048);
        SB0; acc[1][0] = __builtin_amdgcn_mfma_f32_32x32x16_bf16(FW[P][ks][1], FX[P][ks][0], acc[1][0], 0, 0, 0); SB0;
        a2 = ldstr(aw1); a3 = ldstr(aw1 + 1024);
        SB0; acc[1][1] = __builtin_amdgcn_mfma_f32_32x32x16_bf16(FW[P][ks][1], FX[P][ks][1], acc[1][1], 0, 0, 0); SB0;
        FW[Q][ks][0] = __builtin_bit_cast(bf16x8, (u32x4){a0[0], a0[1], a1[0], a1[1]});
        FW[Q][ks][1] = __builtin_bit_cast(bf16x8, (u32x4){a2[0], a2[1], a3[0], a3[1]});
      };
      auto step = [&](auto P_, bool sync) {
        if (sync) rawbar();
        sb = sb == 81920u ? 0u : sb + 16384u;
        half(P_, IC<0>{}); half(P_, IC<1>{});
      };
      waitv<0>();
      rawbar();
      STAMP(1);
#pragma unroll
      for (int ks = 0; ks < 2; ks++)
#pragma unroll
        for (int j = 0; j < 2; j++) {
          FX[0][ks][j] = ldsld<bf16x8>((xa0 ^ (u32)(ks << 5)) + j * 2048);
          u32 a = (wa0 ^ (u32)(j << 5)) + ks * 4096;
          u32x2 a0 = ldstr(a), a1 = ldstr(a + 1024);
          FW[0][ks][j] = __builtin_bit_cast(bf16x8, (u32x4){a0[0], a0[1], a1[0], a1[1]});
        }
#pragma unroll 1
      for (int s = 0; s < 14; s += 2) { step(IC<0>{}, true); step(IC<1>{}, true); }
      step(IC<0>{}, true);
      bar();
#pragma unroll
      for (int ks = 0; ks < 2; ks++)
#pragma unroll
        for (int tau = 0; tau < 2; tau++)
#pragma unroll
          for (int j = 0; j < 2; j++)
            acc[tau][j] = __builtin_amdgcn_mfma_f32_32x32x16_bf16(FW[1][ks][tau], FX[1][ks][j], acc[tau][j], 0, 0, 0);
      STAMP(2);
      wait1(Y_HLP, 1u);
      STAMP(3);
      {
        const int li = lane & 15;
        const u32 fo = (u32)(wf * 64 + 32 * ((li >> 2) & 1) + 8 * blk + 4 * (li >> 3) + (li & 3));
        bf16x8 xa[2], wa[2];
#pragma unroll
        for (int j = 0; j < 2; j++) { u32x2 t = ldsld<u32x2>(hh ? L_ZERO : L_XA + (tl + 32 * j) * 8); xa[j] = __builtin_bit_cast(bf16x8, (u32x4){t[0], t[1], 0u, 0u}); }
#pragma unroll
        for (int tau = 0; tau < 2; tau++) { u32x2 t = ldsld<u32x2>(hh ? L_ZERO : L_WA + (fo + 16 * tau) * 8); wa[tau] = __builtin_bit_cast(bf16x8, (u32x4){t[0], t[1], 0u, 0u}); }
#pragma unroll
        for (int tau = 0; tau < 2; tau++)
#pragma unroll
          for (int j = 0; j < 2; j++) acc[tau][j] = __builtin_amdgcn_mfma_f32_32x32x16_bf16(wa[tau], xa[j], acc[tau][j], 0, 0, 0);
      }
      char* hp = ws + WS_H + (size_t)(bm * 2 + wt) * 229376 + (bn * 16 + wf * 8) * 1024;
#pragma unroll
      for (int j = 0; j < 2; j++) {
        const float r = ldsld<float>(L_RS + (tl + 32 * j) * 4);
        const float c1 = r * 1.44269504088896341f, m2r = -2.f * r;
        const f32x2 c1v = {c1, c1}, rv = {r, r}, m2v = {m2r, m2r}, one = {1.f, 1.f};
#pragma unroll
        for (int tau = 0; tau < 2; tau++) {
#pragma unroll
          for (int hf = 0; hf < 2; hf++) {
            f32x2 av[4], t[4];
#pragma unroll
            for (int jj = 0; jj < 4; jj++) { av[jj] = (f32x2){acc[tau][j][8 * hf + 2 * jj], acc[tau][j][8 * hf + 2 * jj + 1]}; t[jj] = av[jj] * c1v; }
#pragma unroll
            for (int jj = 0; jj < 4; jj++) t[jj] = (f32x2){__builtin_amdgcn_exp2f(t[jj][0]), __builtin_amdgcn_exp2f(t[jj][1])};
#pragma unroll
            for (int jj = 0; jj < 4; jj++) t[jj] = t[jj] + one;
#pragma unroll
            for (int jj = 0; jj < 4; jj++) t[jj] = __builtin_elementwise_fma(t[jj], t[jj], one);
#pragma unroll
            for (int jj = 0; jj < 4; jj++) t[jj] = (f32x2){__builtin_amdgcn_rcpf(t[jj][0]), __builtin_amdgcn_rcpf(t[jj][1])};
#pragma unroll
            for (int jj = 0; jj < 4; jj++) t[jj] = __builtin_elementwise_fma(t[jj], m2v, rv);
            u32x4 o;
#pragma unroll
            for (int jj = 0; jj < 4; jj++) { f32x2 ov = av[jj] * t[jj]; o[jj] = pkbf(ov[0], ov[1]); }
            st16c(hp, (u32)((4 * hh + 2 * tau + hf) * 1024 + ((lane & 31) + 32 * j) * 16), o);
          }
        }
      }
      STAMP(4);
      waitv<0>();
      if (lane == 0) st4c(ws, WS_HFLAG + (u32)((bm * 2 + wt) * 32 + bn * 2 + wf) * 4, EP);
      sig(Y_MD + w * 4, 1u);
      STAMP(5);
    } else {
      bool done = !lead;
      auto lead_check = [&]() {
        u64 m0_, m1_, m2_, m3_;
        asm volatile("v_cmp_ne_u32 %0, %4, v153\n\tv_cmp_ne_u32 %1, %4, v155\n\tv_cmp_ne_u32 %2, %4, v157\n\tv_cmp_ne_u32 %3, %4, v159"
                     : "=&s"(m0_), "=&s"(m1_), "=&s"(m2_), "=&s"(m3_) : "s"(cand) : "memory");
        if ((m0_ | m1_ | m2_ | m3_) != 0) return false;
        u32x4 o;
        asm volatile("s_waitcnt vmcnt(0)\n\tv_mov_b32 %0, v152\n\tv_mov_b32 %1, v154\n\tv_mov_b32 %2, v156\n\tv_mov_b32 %3, v158"
                     : "=&v"(o[0]), "=&v"(o[1]), "=&v"(o[2]), "=&v"(o[3])::"memory");
        st16c(ws + WS_A, (u32)lane * 16, o);
        return true;
      };
      waitv<0>();
      if (lead) LEAD_POLL();
      rawbar();
      if (ci < 28) {
        float cs[8];
#pragma unroll
        for (int e = 0; e < 8; e++) cs[e] = 0.f;
#pragma unroll 4
        for (int j = 0; j < 16; j++) {
          u32x4 cw = ldsld<u32x4>((u32)(j * 4096 + tid * 16));
#pragma unroll
          for (int e = 0; e < 4; e++) { cs[2 * e] += bf2f(cw[e]); cs[2 * e + 1] += bfhi(cw[e]); }
        }
        ldsst<f32x4>((u32)(65536 + tid * 32), (f32x4){cs[0], cs[1], cs[2], cs[3]});
        ldsst<f32x4>((u32)(65536 + tid * 32 + 16), (f32x4){cs[4], cs[5], cs[6], cs[7]});
      }
      bar();
      if (ci < 28 && w == 0) {
        float s = 0.f;
#pragma unroll 8
        for (int r = 0; r < 32; r++) s += ldsld<float>((u32)(65536 + r * 256 + lane * 4));
        st8c(ws, WS_CS + (u32)(ci * 512 + lane * 8), (u32x2){__float_as_uint(s), EP});
      }
#pragma unroll 1
      for (int s = 1; s < 16; s++) {
        if (!done) { done = lead_check(); if (!done) LEAD_POLL(); else STAMP(11); }
        bar();
      }
      u32 spins = 0;
      while (!done && ++spins < SPIN_MAX) { LEAD_POLL(); waitv<0>(); done = lead_check(); }
      if (lead && spins) STAMP(11);
    }
    sb = 65536u;
    const int wd = w & 1, wt2 = w >> 1;
    const int tl = wt2 * 32 + (lane & 31);
    const u32 ha0 = (u32)(hh * 1024 + tl * 16);
    const u32 wa0 = (u32)(8192 + (8 * hh + q4) * 128 + (((4 * wd + 2 * (p4 & 1) + blk) ^ ((q4 & 2) << 1)) << 4) + 8 * (p4 >> 1));
    const int dl = wd * 32 + 16 * hh;
    f32x16 acc;
#pragma unroll
    for (int i = 0; i < 16; i++) acc[i] = 0.f;
    bf16x8 FH[2][4], FW[2][4];
    auto rdf2 = [&](auto P_, int ks) {
      constexpr int P = decltype(P_)::value;
      FH[P][ks] = ldsld<bf16x8>(sb + ha0 + ks * 2048);
      u32 a = sb + wa0 + ks * 2048;
      u32x2 a0 = ldstr(a), a1 = ldstr(a + 512);
      FW[P][ks] = __builtin_bit_cast(bf16x8, (u32x4){a0[0], a0[1], a1[0], a1[1]});
    };
    auto step2 = [&](auto P_) {
      constexpr int P = decltype(P_)::value;
      rawbar();
      sb = sb == 81920u ? 0u : sb + 16384u;
#pragma unroll
      for (int ks = 0; ks < 4; ks++) {
        SB0; acc = __builtin_amdgcn_mfma_f32_32x32x16_bf16(FW[P][ks], FH[P][ks], acc, 0, 0, 0); SB0;
        rdf2(IC<1 - P>{}, ks);
      }
    };
    __builtin_amdgcn_s_setprio(3);
    bar();
    STAMP(6);
    rawbar();
    STAMP(7);
#pragma unroll
    for (int ks = 0; ks < 4; ks++) rdf2(IC<0>{}, ks);
#pragma unroll 1
    for (int s = 0; s < 26; s += 2) { step2(IC<0>{}); step2(IC<1>{}); }
    step2(IC<0>{});
#pragma unroll
    for (int ks = 0; ks < 4; ks++) acc = __builtin_amdgcn_mfma_f32_32x32x16_bf16(FW[1][ks], FH[1][ks], acc, 0, 0, 0);
    STAMP(8);
    rawbar();
    u32 ya = L_YT + (u32)((dl + q4) * 128 + (p4 >> 1) * 64 + (wt2 * 2 + blk) * 16 + (p4 & 1) * 8);
#pragma unroll
    for (int hf = 0; hf < 2; hf++) {
      u32x2 y0 = ldstr(ya + hf * 1024), y1 = ldstr(ya + hf * 1024 + 512);
      u32x4 yy = {y0[0], y0[1], y1[0], y1[1]};
      const u32 oa = L_OUT + (u32)(tl * 128 + (((4 * wd + 2 * hh + hf) ^ (tl & 7)) << 4));
      u32x4 xv = ldsld<u32x4>(oa);
      u32x4 ov;
#pragma unroll
      for (int j = 0; j < 4; j++) {
        u32 s1 = pkbf(bf2f(xv[j]) + bf2f(yy[j]), bfhi(xv[j]) + bfhi(yy[j]));
        u32 s2 = pkbf(acc[8 * hf + 2 * j], acc[8 * hf + 2 * j + 1]);
        ov[j] = pkbf(bf2f(s1) + bf2f(s2), bfhi(s1) + bfhi(s2));
      }
      ldsst<u32x4>(oa, ov);
    }
#pragma unroll
    for (int j = 0; j < 2; j++) {
      int t = wt2 * 32 + (lane >> 2) + 16 * j, c = 4 * wd + (lane & 3);
      u32x4 ov = ldsld<u32x4>(L_OUT + (u32)(t * 128 + ((c ^ (t & 7)) << 4)));
      st16((char*)out + (size_t)bm2 * 65536 + bn2 * 128, (u32)(t * 1024 + c * 16), ov);
    }
    STAMP(9);
  } else {
    const int lw = w - 4, lt = tid - 256;
    const int G = lw >> 2, kch = (lw >> 1) & 1, kmh = lw & 1;
    const int dch = 2 * tb + kch;
    const bool shk = lw == 6;
    u32 pa, pb, pc_, pd, pe, pf, pl, pk_;
    {
      u32 po = (u32)((dch * 128 + kmh * 64 + lane) * 2);
      u32 lo = (u32)(dch * 2);
      asm volatile(
          "global_load_ushort %0, %8, %10\n\t"
          "global_load_ushort %1, %8, %11\n\t"
          "global_load_ushort %2, %8, %12\n\t"
          "global_load_ushort %3, %8, %13\n\t"
          "global_load_ushort %4, %8, %14\n\t"
          "global_load_ushort %5, %8, %15\n\t"
          "global_load_ushort %6, %9, %16\n\t"
          "global_load_ushort %7, %9, %17"
          : "=&v"(pa), "=&v"(pb), "=&v"(pc_), "=&v"(pd), "=&v"(pe), "=&v"(pf), "=&v"(pl), "=&v"(pk_)
          : "v"(po), "v"(lo), "s"(Are), "s"(Aim), "s"(Bre), "s"(Bim), "s"(Cre), "s"(Cim), "s"(ldt), "s"(Dsk)
          : "memory");
    }
    const u32 ao = lane == 0 ? WS_N : WS_A + (u32)b * 4;
#define SHK_POLL() asm volatile("global_load_dword v165, %0, %1 sc1" ::"v"(ao), "s"(ws) : "memory", "v165")
    if (shk) {
      const u32 oy = WS_YFLAG + (u32)((lane & 1) * 512 + 2 * tb + ((lane >> 1) & 1)) * 4;
      const u32 oh = WS_HFLAG + (u32)((bm * 2 + ((lane >> 1) & 1)) * 32 + bn * 2 + (lane & 1)) * 4;
      u32 off = lane < 4 ? oy : ((lane < 8 && isg1) ? oh : WS_UFLAG + (u32)tb * 4);
      if (lane < 9) st4c(ws, off, 0u);
      if (!isg1 && ci < 28) st4c(ws, WS_CS + (u32)(ci * 512 + lane * 8 + 4), 0u);
      SHK_POLL();
    }
    const u32 rot2 = (u32)((bi * 9) % 28), rot1 = (u32)(2 * ((2 * bm + bn) & 7));
    const int wp = lw < 4 ? lw : 2 * lw - 4;
    u32 v2, vX, vW;
    {
      const int row2 = 8 * wp + (lane >> 3);
      v2 = (u32)(row2 * 1024 + (((lane & 7) ^ ((row2 & 2) << 1)) << 4) + bn2 * 128);
      int row = 32 * lw + (lane >> 2);
      vX = (u32)((bm * 128 + row) * 1024 + (((lane & 3) ^ ((row >> 2) & 3)) << 4));
      int k = 4 * wp + (lane >> 4);
      vW = (u32)(k * 3584 + (((lane & 15) ^ (((k & 1) << 1) | ((k & 2) << 2))) << 4) + bn * 256);
    }
    const u32x4 sW2 = mksrd(w2, 1792u * 1024u), sX = mksrd(x, 2048u * 1024u), sW1 = mksrd(w1, 512u * 3584u);
    u32 so2 = rot2 * 65536u, soX = rot1 * 64u, soW = rot1 * 114688u, sl = 0;
    const u32 la = (u32)(lw * 2048), lbw = (u32)(8192 + wp * 1024);
    auto nextsl = [&]() { sl += 16384u; sl = sl == 98304u ? 0u : sl; };
    auto next2 = [&]() { so2 += 65536u; so2 = so2 == 28u * 65536u ? 0u : so2; };
    auto i1 = [&](auto T_) {
      if constexpr (decltype(T_)::value == 0) { dma21<false>(sX, soX, vX, vX + 15360u, sW1, soW, vW, sl + la, sl + lbw); soX = (soX + 64u) & 1023u; }
      else dma2<false>(sW1, soW, vW, vW + 13312u, sl + lbw);
      soW += 114688u; soW = soW == 16u * 114688u ? 0u : soW;
      nextsl();
    };
    auto iw2 = [&](auto T_) {
      if constexpr (decltype(T_)::value == 0) dma1<false>(sW2, so2, v2, sl + lbw); else dma2<false>(sW2, so2, v2, v2 + 7168u, sl + lbw);
      next2(); nextsl();
    };
    auto i1n = [&](int n) {
      if (lw < 4) { for (int q = 0; q < n; q++) i1(IC<0>{}); } else if (lw < 6) { for (int q = 0; q < n; q++) i1(IC<1>{}); }
    };
    const int ech = lt >> 8, eg = lt & 255;
    const u32 pb0 = L_TAB + (u32)ech * 16384;
    u32 ad[12];
    {
      const float lf = (float)(2047 - 8 * eg);
      const u32 cb0 = pb0 - 0x3f00u * 4u;
#pragma unroll
      for (int e = 0; e < 12; e += 2) {
        u32 pk2 = pkbf(fmaxf(lf - (float)e, 0.5f), fmaxf(lf - (float)(e + 1), 0.5f));
        ad[e] = cb0 + ((pk2 & 0xffffu) << 2);
        ad[e + 1] = cb0 + ((pk2 >> 16) << 2);
        asm volatile("" : "+v"(ad[e]), "+v"(ad[e + 1]));
      }
    }
    if (shk) waitv<2>(); else waitv<0>();
    asm volatile("" : "+v"(pa), "+v"(pb), "+v"(pc_), "+v"(pd), "+v"(pe), "+v"(pf), "+v"(pl), "+v"(pk_)::"memory");
    STAMP(1);
    if (isg1) i1n(3);
    else if (ci < 28 && lw < 4) {
      const u32 vc = (u32)((16 * lw + (lane >> 3)) * 3584 + (lane & 7) * 16 + ci * 128);
#pragma unroll 1
      for (int j = 0; j < 4; j++) dma2<false>(sW1, (u32)j * 64u * 3584u, vc, vc + 8u * 3584u - 1024u, (u32)(j * 8192 + lw * 2048));
    }
    u32 rb_ = 0, nsent = 0;
    bool rel = false;
    auto shk_step = [&]() {
      u32 n, a;
      asm volatile("v_readlane_b32 %0, v165, 0\n\tv_readlane_b32 %1, v165, 1" : "=s"(n), "=s"(a)::"memory");
      if (a == rb_) rel = true;
      else {
        if (n != nsent) { nsent = n; if (lane == 0) st8c(ws, WS_R + (u32)b * 8, (u32x2){rb_, n}); }
        SHK_POLL();
      }
    };
    const int rr = lane & 15, kg = lane >> 4;
    float kinv, kbound;
    f32x4 d0 = {0.f, 0.f, 0.f, 0.f}, d1 = {0.f, 0.f, 0.f, 0.f};
    u32 pad[4], pbase;
    {
      float dt = rbf(expf(bf2f(pl)));
      float a2 = (dt * bf2f(pa)) * 1.44269504088896341f;
      float bb = dt * bf2f(pb);
      f32x2 W = cmul((f32x2){bf2f(pe), bf2f(pf)}, (f32x2){dt * bf2f(pc_), dt * bf2f(pd)});
      const float C_HI = 0.15915494309189535f, C_LO = (float)(0.15915494309189533577 - (double)0.15915494309189535f);
      float p = bb * C_HI;
      float er = __builtin_fmaf(bb, C_HI, -p);
      float fhi = __uint_as_float(__float_as_uint(p) & 0xfffff000u);
      float flo = (p - fhi) + __builtin_fmaf(bb, C_LO, er);
      float g = 0.f;
      if (__any(a2 > 0.f)) { g = wave_max0(fmaxf(a2, 0.f)); a2 -= g; }
      float wsum = wave_sum(fabsf(W[0]) + fabsf(W[1]));
      float sig_, isig;
      pow2scale(wsum, 13, sig_, isig);
      const f32x2 W0 = {W[0] * sig_, -W[1] * sig_};
      const u32 T = L_TAB + lw * 8192;
      const u32 wbase = T + (lane >> 2) * 16 + (lane & 3) * 4;
      const u32 ra = T + rr * 256 + ((kg ^ rr) << 4);
      const float cs = G ? 8.f : 2.f, rs = G ? 128.f : 32.f, rb = G ? 512.f : 0.f;
      f32x2 zc = cpow(a2, -fhi, -flo, cs), zr = cpow(a2, fhi, flo, rs);
      f32x2 zc4 = cpow(a2, -fhi, -flo, 4.f * cs), zr4 = cpow(a2, fhi, flo, 4.f * rs);
      f32x2 zb = cpow(a2, fhi, flo, rb), zx = cpow(a2, fhi, flo, 0.5f * cs);
      f32x2 zc2 = cmul(zc, zc), zr2 = cmul(zr, zr);
      f32x2 c[4], r[4], e0[4], e1[4];
      c[0] = W0; c[1] = cmul(W0, zc); r[0] = zb; r[1] = cmul(zb, zr);
      c[2] = cmul(c[0], zc2); c[3] = cmul(c[1], zc2); r[2] = cmul(r[0], zr2); r[3] = cmul(r[1], zr2);
      SB0;
      if (shk) waitv<0>();
      rawbar();
      if (shk) {
        u32 n, a;
        asm volatile("v_readlane_b32 %0, v165, 0\n\tv_readlane_b32 %1, v165, 1" : "=s"(n), "=s"(a)::"memory");
        rb_ = a + 1u; nsent = n;
        if (lane == 0) st8c(ws, WS_R + (u32)b * 8, (u32x2){rb_, n});
        SHK_POLL();
      }
      if (!isg1 && ci < 28 && lw < 4) {
        const u32 vc = (u32)((16 * lw + (lane >> 3)) * 3584 + (lane & 7) * 16 + ci * 128);
#pragma unroll 1
        for (int j = 4; j < 8; j++) dma2<false>(sW1, (u32)j * 64u * 3584u, vc, vc + 8u * 3584u - 1024u, (u32)(j * 8192 + lw * 2048));
      }
      SB0;
#pragma unroll
      for (int o = 0; o < 4; o++) {
        if (o > 0) { cmul4(c[0], c[1], c[2], c[3], zc4); cmul4(r[0], r[1], r[2], r[3], zr4); }
        if (o == 0) { e0[0] = r[0]; e0[1] = r[1]; e0[2] = r[2]; e0[3] = r[3]; cmul4(e0[0], e0[1], e0[2], e0[3], zx); }
        if (o == 1) { e1[0] = r[0]; e1[1] = r[1]; e1[2] = r[2]; e1[3] = r[3]; cmul4(e1[0], e1[1], e1[2], e1[3], zx); }
#pragma unroll
        for (int j = 0; j < 4; j++) {
          int e = 4 * o + j;
          u32 a = wbase ^ (u32)(e << 4);
          ldsst<u32>(a + e * 256, pkh(r[j][0], r[j][1]));
          ldsst<u32>(a + e * 256 + 4096, pkh(c[j][0], c[j][1]));
        }
      }
      f16x8 af[4], bf[4];
#pragma unroll
      for (int s = 0; s < 4; s++) { af[s] = ldsld<f16x8>(ra ^ (u32)(s << 6)); bf[s] = ldsld<f16x8>((ra ^ (u32)(s << 6)) + 4096); }
#pragma unroll
      for (int j = 0; j < 4; j++) {
        ldsst<u32>((wbase ^ (u32)(j << 4)) + j * 256, pkh(e0[j][0], e0[j][1]));
        ldsst<u32>((wbase ^ (u32)((4 + j) << 4)) + (4 + j) * 256, pkh(e1[j][0], e1[j][1]));
      }
      f16x8 ag[4];
#pragma unroll
      for (int s = 0; s < 4; s++) ag[s] = ldsld<f16x8>(ra ^ (u32)(s << 6));
      SB0;
      if (isg1) i1n(2);
      if (shk) shk_step();
#pragma unroll
      for (int s = 0; s < 4; s++) d0 = __builtin_amdgcn_mfma_f32_16x16x32_f16(af[s], bf[s], d0, 0, 0, 0);
#pragma unroll
      for (int s = 0; s < 4; s++) d1 = __builtin_amdgcn_mfma_f32_16x16x32_f16(ag[s], bf[s], d1, 0, 0, 0);
      const float l00 = rb + rs * (float)(4 * kg) + cs * (float)rr;
      const u32 PB = L_TAB + (u32)(lw & 3) * 8192 - 0x3f00u * 4u;
#pragma unroll
      for (int e = 0; e < 4; e++) {
        float l0 = l00 + rs * (float)e, l1 = l0 + 0.5f * cs;
        float s0 = isig, s1 = isig;
        if (g > 0.f) { s0 *= __builtin_amdgcn_exp2f(g * l0); s1 *= __builtin_amdgcn_exp2f(g * l1); }
        l0 = fmaxf(l0, 0.5f);
        l0 = l0 > 2048.f ? 4096.f : l0;
        l1 = l1 < 128.f * cs ? l1 : 4096.f;
        pad[e] = (__float_as_uint(l0) >> 16) | (__float_as_uint(l1) & 0xffff0000u);
        d0[e] *= s0; d1[e] *= s1;
      }
      pbase = PB;
      kbound = wsum * __builtin_amdgcn_exp2f(g * 2048.f);
      if (lw == 7 && lane < 32) ldsst<u32>(L_ZERO + lane * 4, 0u);
    }
    STAMP(2);
    auto put_partials = [&]() {
#pragma unroll
      for (int e = 0; e < 4; e++) {
        ldsst<float>(pbase + ((pad[e] & 0xffffu) << 2), d0[e]);
        ldsst<float>(pbase + ((pad[e] >> 16) << 2), d1[e]);
      }
      if (lane == 0 && G == 0) ldsst<float>(L_TAB + lw * 8192 + 7168, kbound);
      sig(Y_EXP + lw * 4, 1u);
    };
    auto expand = [&]() {
      wait8(Y_EXP, 1u);
      float bnd = ldsld<float>(pb0 + 7168) + ldsld<float>(pb0 + 8192 + 7168);
      float bnd2 = ldsld<float>(L_TAB + kch * 16384 + 7168) + ldsld<float>(L_TAB + kch * 16384 + 8192 + 7168);
      float v[11];
#pragma unroll
      for (int e = 0; e < 11; e++) v[e] = ldsld<float>(ad[e]) + ldsld<float>(ad[e] + 8192);
      sig(Y_EXP + lw * 4, 2u);
      float ks, ki, ks2;
      pow2scale(bnd, 14, ks, ki);
      pow2scale(bnd2, 14, ks2, kinv);
      u32 PK[5], QK[5];
#pragma unroll
      for (int e = 0; e < 11; e++) v[e] *= (e >= 8 && eg == 255) ? 0.f : ks;
#pragma unroll
      for (int j = 0; j < 5; j++) { PK[j] = pkh(v[2 * j], v[2 * j + 1]); QK[j] = pkh(v[2 * j + 1], v[2 * j + 2]); }
      wait8(Y_EXP, 2u);
      u32 a = L_KCP + ech * 16640 + eg * 16;
      ldsst<u32x4>(a, (u32x4){PK[0], PK[1], PK[2], PK[3]});
      ldsst<u32x4>(a + 4160, (u32x4){QK[0], QK[1], QK[2], QK[3]});
      ldsst<u32x4>(a + 8320, (u32x4){PK[1], PK[2], PK[3], PK[4]});
      ldsst<u32x4>(a + 12480, (u32x4){QK[1], QK[2], QK[3], QK[4]});
      if (lt < 16) ldsst<u32x4>(L_KCP + (lt >> 1) * 4160 + (256 + (lt & 1)) * 16, (u32x4){0, 0, 0, 0});
      sig(Y_EXP + lw * 4, 3u);
    };
    SB0;
    __builtin_amdgcn_s_setprio(2);
    if (!isg1) waitv<0>(); else if (lw < 4) waitv<6>(); else if (lw < 6) waitv<4>();
    bar();
    if (isg1) i1n(1);
    put_partials();
    const u32x4 sH = mksrd(ws + WS_H + (size_t)bm2 * 229376, 229376u);
    const u32 vH = (u32)(lw * 2048 + lane * 16);
    u32 soH = rot2 * 8192u;
    auto nextH = [&]() { soH += 8192u; soH = soH == 229376u ? 0u : soH; };
    const int ya_ = bm2 >> 2;
    if (lw < 6) {
      auto run = [&](auto T_) {
        constexpr int T = decltype(T_)::value, N = T ? 2 : 3, NW = T ? 2 : 1;
        if (isg1) {
          rawbar();
          rawbar(); i1(T_);
#pragma unroll 1
          for (int s = 2; s <= 10; s++) { waitv<3 * N>(); rawbar(); i1(T_); }
          waitv<3 * N>(); rawbar(); iw2(T_); waitv<2 * N + NW>(); rawbar(); iw2(T_);
          waitv<N + 2 * NW>(); rawbar(); iw2(T_); waitv<3 * NW>(); rawbar(); iw2(T_);
          bar();
          iw2(T_); iw2(T_);
        } else {
#pragma unroll 1
          for (int s = 0; s < 16; s++) bar();
          sl = 65536u;
#pragma unroll 1
          for (int q = 0; q < 6; q++) iw2(T_);
        }
        STAMP(8);
        expand();
        STAMP(9);
        if constexpr (T == 0) {
          wait8(Y_EXP, 3u);
          wait1(Y_UOK, 1u);
          STAMP(3);
          const int n = rr;
          f32x4 acc[8];
#pragma unroll
          for (int a = 0; a < 8; a++) acc[a] = (f32x4){0.f, 0.f, 0.f, 0.f};
          const int cc = (3 - n) & 3;
          u32 ub = L_UL + kch * 4608 + (u32)((256 + 16 * (n - 8 * kmh) - 16 + 8 * kg) * 2);
          u32 ab = L_KCP + kch * 16640 + cc * 4160 + (u32)((2031 - n + 8 * kg - cc - 1792 - 128 * kmh) * 2);
          f16x8 Bq[8];
          u32x2 Al[4], Ah[4];
#pragma unroll
          for (int q = 0; q < 8; q++) Bq[q] = ldsld<f16x8>(ub + q * 512);
#pragma unroll
          for (int i = 0; i < 4; i++) { Al[i] = ldsldv<u32x2>(ab + (7 - i) * 512); Ah[i] = ldsldv<u32x2>(ab + (7 - i) * 512 + 8); }
#pragma unroll 1
          for (int it = 0; it < 4; it++) {
#pragma unroll
            for (int i = 0; i < 8; i++) {
              const int in = (i + 4) & 7;
              u32 a = ab + (7 - in) * 512 - (i >= 4 ? 64 : 0);
              f16x8 A = __builtin_bit_cast(f16x8, (u32x4){Al[i & 3][0], Al[i & 3][1], Ah[i & 3][0], Ah[i & 3][1]});
#pragma unroll
              for (int a2 = 7; a2 >= i; a2--) acc[a2] = __builtin_amdgcn_mfma_f32_16x16x32_f16(A, Bq[a2 - i], acc[a2], 0, 0, 0);
              SB0;
              Al[i & 3] = ldsldv<u32x2>(a); Ah[i & 3] = ldsldv<u32x2>(a + 8);
              Bq[7 - i] = ldsld<f16x8>(ub - 64 + (7 - i) * 512);
              SB0;
            }
            ub -= 64; ab -= 64;
          }
          STAMP(4);
          if (kmh) {
#pragma unroll
            for (int a = 0; a < 4; a++) { f32x4 t = acc[a]; acc[a] = acc[4 + a]; acc[4 + a] = t; asm volatile("" : "+v"(acc[a]), "+v"(acc[4 + a])); }
          }
#pragma unroll
          for (int a = 0; a < 4; a++) ldsst<f32x4>(L_RED + (u32)(lw * 4096 + a * 1024 + lane * 16), acc[4 + a]);
          sig(Y_BST + lw * 4, 1u);
          wait1(Y_BST + (lw ^ 1) * 4, 1u);
          const float dsk = bf2f(pk_);
#pragma unroll
          for (int pr = 0; pr < 2; pr++) {
            u32 pk[2][2];
#pragma unroll
            for (int j = 0; j < 2; j++) {
              const int a = 2 * pr + j;
              f32x4 yy = acc[a] + ldsld<f32x4>(L_RED + (u32)((lw ^ 1) * 4096 + a * 1024 + lane * 16));
              f16x4 uu = ldsld<f16x4>(L_UL + kch * 4608 + 512 + (u32)((256 * (4 * kmh + a) + 16 * n + 4 * kg) * 2));
              u32 y01 = pkbf(yy[0] * kinv, yy[1] * kinv), y23 = pkbf(yy[2] * kinv, yy[3] * kinv);
              u32 s01 = pkbf((float)uu[0] * dsk, (float)uu[1] * dsk), s23 = pkbf((float)uu[2] * dsk, (float)uu[3] * dsk);
              pk[j][0] = pkbf(bf2f(y01) + bf2f(s01), bfhi(y01) + bfhi(s01));
              pk[j][1] = pkbf(bf2f(y23) + bf2f(s23), bfhi(y23) + bfhi(s23));
            }
            auto s0 = __builtin_amdgcn_permlane16_swap(pk[0][0], pk[1][0], false, false);
            auto s1 = __builtin_amdgcn_permlane16_swap(pk[0][1], pk[1][1], false, false);
            ldsst<u32x4>(L_RED + (u32)((lw ^ 1) * 4096 + (2 * pr + (kg & 1)) * 512 + (kg >> 1) * 256 + n * 16), (u32x4){s0[0], s1[0], s0[1], s1[1]});
          }
          sig(Y_BST + lw * 4, 2u);
          STAMP(5);
        }
        bar();
        STAMP(6);
        auto i2 = [&]() {
          if constexpr (T == 0) { dma21<true>(sH, soH, vH, vH, sW2, so2, v2, sl + la, sl + lbw); nextH(); }
          else dma2<false>(sW2, so2, v2, v2 + 7168u, sl + lbw);
          next2(); nextsl();
        };
        if constexpr (T == 0) {
#pragma unroll 1
          for (int q = 0; q < 6; q++) { dma2<true>(sH, soH, vH, vH, sl + la); nextH(); nextsl(); }
          waitv<10>();
          rawbar();
          waitv<8>(); rawbar();
          waitv<6>(); rawbar(); i2(); waitv<7>(); rawbar(); i2(); waitv<8>(); rawbar(); i2();
        } else {
          waitv<0>();
          rawbar(); rawbar();
          rawbar(); i2(); rawbar(); i2(); rawbar(); i2();
        }
#pragma unroll 1
        for (int s = 4; s < 23; s++) { waitv<3 * N>(); rawbar(); i2(); }
        waitv<3 * N>(); rawbar(); waitv<2 * N>(); rawbar(); waitv<N>(); rawbar(); waitv<0>(); rawbar();
        rawbar();
      };
      if (lw < 4) run(IC<0>{}); else run(IC<1>{});
    } else if (shk) {
      const char* sb_ = ws + WS_STAT + bm * 1024;
      const char* cb_ = ws + WS_CS + bn * 1024;
#define HLP_POLL() asm volatile("global_load_dwordx4 v[152:155], %0, %1 sc1\n\tglobal_load_dwordx4 v[156:159], %0, %2 sc1" \
                                ::"v"((u32)lane * 16), "s"(sb_), "s"(cb_) : "memory", "v152", "v153", "v154", "v155", "v156", "v157", "v158", "v159")
      if (isg1) asm volatile("global_load_dword v163, %0, %1" ::"v"((u32)lane * 4), "s"(b1 + bn * 128) : "memory", "v163");
      if (lane == 0) st4c(ws, WS_UFLAG + (u32)tb * 4, EP);
      int sst = 0;
      auto svc = [&]() {
        if (sst == 0) {
          if (!rel) shk_step();
          if (rel) {
            sig(Y_REL, 1u);
            sst = isg1 ? 1 : 100;
            if (isg1) { asm volatile("v_mov_b32 v153, 0\n\tv_mov_b32 v157, 0" ::: "v153", "v157"); HLP_POLL(); }
          }
        } else if (sst < 100) {
          u64 m0_, m1_, m2_, m3_;
          asm volatile("v_cmp_ne_u32 %0, 1, v153\n\tv_cmp_ne_u32 %1, 1, v155\n\tv_cmp_ne_u32 %2, 1, v157\n\tv_cmp_ne_u32 %3, 1, v159"
                       : "=&s"(m0_), "=&s"(m1_), "=&s"(m2_), "=&s"(m3_)::"memory");
          if ((m0_ | m1_ | m2_ | m3_) == 0) {
            u32 g0, g1, c0, c1, bv;
            asm volatile("v_mov_b32 %0, v152\n\tv_mov_b32 %1, v154\n\tv_mov_b32 %2, v156\n\tv_mov_b32 %3, v158\n\tv_mov_b32 %4, v163"
                         : "=&v"(g0), "=&v"(g1), "=&v"(c0), "=&v"(c1), "=&v"(bv)::"memory");
            u32 n0 = (g0 & 0xffffu) ^ 0x8000u, n1 = (g1 & 0xffffu) ^ 0x8000u;
            ldsst<u32x4>(L_XA + lane * 16, (u32x4){n0 | (n0 << 16), g0 >> 16, n1 | (n1 << 16), g1 >> 16});
            ldsst<f32x2>(L_RS + lane * 8, (f32x2){1.f / bfhi(g0), 1.f / bfhi(g1)});
            float f0 = __uint_as_float(c0), f1 = __uint_as_float(c1), h0 = rbf(f0), h1 = rbf(f1);
            ldsst<u32x4>(L_WA + lane * 16, (u32x4){pkbf(h0, f0 - h0), bv & 0xffffu, pkbf(h1, f1 - h1), bv >> 16});
            sig(Y_HLP, 1u);
            sst = 100;
          } else HLP_POLL();
        }
      };
#pragma unroll 1
      for (int s = 0; s < 16; s++) { if (s & 1) svc(); rawbar(); }
      svc();
      expand();
      u32 spins = 0;
      while (sst < 100 && ++spins < SPIN_MAX) { waitv<0>(); svc(); }
      sig(Y_REL, 1u); sig(Y_HLP, 1u);
      if (isg1) wait4(Y_MD, 1u);
      STAMP(3);
      spins = 0;
      for (;;) {
        u32 f = ld4c_wait(ws + WS_HFLAG + bm2 * 128, (u32)(lane < 28 ? lane : 0) * 4);
        if (__all(f == EP) || ++spins > SPIN_MAX) break;
        __builtin_amdgcn_s_sleep(10);
      }
      STAMP(4);
      bar();
    } else {
#define UG_POLL() asm volatile("global_load_dwordx4 v[152:155], %0, %1 sc1" ::"v"((u32)lane * 16), "s"(ws + WS_UFLAG) : "memory", "v152", "v153", "v154", "v155")
      ldsst<u32x4>(L_UL + (lane >> 5) * 4608 + (lane & 31) * 16, (u32x4){0, 0, 0, 0});
      int sst = 0;
      auto svc = [&]() {
        if (sst == 0) {
          if (__builtin_amdgcn_readfirstlane(ldsldv<u32>(Y_REL))) { asm volatile("v_mov_b32 v152, 0" ::: "v152"); UG_POLL(); sst = 1; }
        } else if (sst < 100) {
          u64 m0_, m1_, m2_, m3_;
          asm volatile("v_cmp_ne_u32 %0, 1, v152\n\tv_cmp_ne_u32 %1, 1, v153\n\tv_cmp_ne_u32 %2, 1, v154\n\tv_cmp_ne_u32 %3, 1, v155"
                       : "=&s"(m0_), "=&s"(m1_), "=&s"(m2_), "=&s"(m3_)::"memory");
          if ((m0_ | m1_ | m2_ | m3_) == 0) {
            u32 vo = (u32)lane * 8192;
#pragma unroll
            for (int j = 0; j < 4; j++)
              glds2<4608, true>(ws + WS_U + (size_t)tb * 32 + (size_t)j * 524288, vo, vo + 16, L_UL + 512 + (u32)j * 1024);
            sst = 100;
          } else UG_POLL();
        }
      };
#pragma unroll 1
      for (int s = 0; s < 16; s++) { if (!(s & 1)) svc(); rawbar(); }
      svc();
      expand();
      u32 spins = 0;
      while (sst < 100 && ++spins < LSPIN_MAX) { waitv<0>(); svc(); }
      STAMP(3);
      waitv<0>();
      sig(Y_UOK, 1u);
      STAMP(4);
      wait4(Y_BST, 2u);
#pragma unroll
      for (int j = 0; j < 8; j++) {
        u32x4 yv = ldsld<u32x4>(L_RED + (u32)(((j >> 1) ^ 1) * 4096 + (j & 1) * 1024 + lane * 16));
        st16c(ws + WS_Y + (size_t)tb * 8192, (u32)(j * 1024 + lane * 16), yv);
      }
      bar();
      {
        const char* xb2 = (const char*)x + (size_t)bm2 * 65536 + bn2 * 128;
        u32 vx = (u32)((lane >> 3) * 1024 + (((lane & 7) ^ (lane >> 3)) << 4));
        glds8<16384, false>(xb2, vx, vx + 8192, L_OUT);
      }
      const char* yfb = ws + WS_YFLAG + ((ya_ >> 2) * 512 + bn2 * 64) * 4;
      bool got = false;
      auto ytile = [&]() {
        const char* yb = ws + WS_Y + (size_t)bn2 * 64 * 4096 + ya_ * 512 + (bm2 & 3) * 64;
        u32 vo = (u32)((lane >> 3) * 4096 + ((lane >> 2) & 1) * 256 + (lane & 3) * 16);
        glds8<65536, true>(yb, vo, vo + 32768, L_YT);
      };
      asm volatile("v_mov_b32 v164, 0" ::: "v164");
#pragma unroll 1
      for (int it = 0; it < 28; it++) {
        if (it == 6) {
          waitv<0>();
          if (lane < 4) st4c(ws, WS_YFLAG + (u32)((lane & 1) * 512 + 2 * tb + (lane >> 1)) * 4, EP);
        }
        if (!got && it >= 12) {
          u64 ne;
          asm volatile("v_cmp_ne_u32 %0, 1, v164" : "=s"(ne)::"memory");
          if (ne == 0) { got = true; ytile(); }
          else if ((it & 3) == 0) asm volatile("global_load_dword v164, %0, %1 sc1" ::"v"((u32)lane * 4), "s"(yfb) : "memory", "v164");
        }
        rawbar();
      }
      if (!got) {
        u32 yf;
        spins = 0;
        waitv<0>();
        do { yf = ld4c_wait(yfb, (u32)lane * 4); } while (!__all(yf == EP) && ++spins < SPIN_MAX);
        ytile();
      }
      waitv<0>();
      rawbar();
    }
    STAMP(7);
  }
  STAMP_OUT();
}

extern "C" void kernel_launch(void* const* d_in, const int* in_sizes, int n_in, void* d_out, int out_size, void* d_ws,
                              size_t ws_size, hipStream_t stream) {
  fused_kernel<<<256, 768, 0, stream>>>((const u16*)d_in[0], (const u16*)d_in[1], (const u16*)d_in[2], (const u16*)d_in[3],
                                        (const u16*)d_in[4], (const u16*)d_in[5], (const u16*)d_in[6], (const u16*)d_in[7],
                                        (const u16*)d_in[8], (const u16*)d_in[10], (const u16*)d_in[11], (const u16*)d_in[12],
                                        (u16*)d_out, (char*)d_ws);
}
```

```cpp
#include <hip/hip_runtime.h>
#include <stdint.h>
#include <type_traits>

typedef unsigned short u16;
typedef unsigned int u32;
typedef unsigned long long u64;
typedef __attribute__((ext_vector_type(8))) __bf16 bf16x8;
typedef __attribute__((ext_vector_type(2))) __bf16 bf16x2;
typedef __attribute__((ext_vector_type(8))) _Float16 f16x8;
typedef __attribute__((ext_vector_type(4))) _Float16 f16x4;
typedef __attribute__((ext_vector_type(2))) _Float16 f16x2;
typedef __attribute__((ext_vector_type(2))) float f32x2;
typedef __attribute__((ext_vector_type(4))) float f32x4;
typedef __attribute__((ext_vector_type(16))) float f32x16;
typedef __attribute__((ext_vector_type(4))) u32 u32x4;
typedef __attribute__((ext_vector_type(2))) u32 u32x2;
typedef __attribute__((ext_vector_type(4))) short s16x4;
#define AS3 __attribute__((address_space(3)))
#define DEVI __device__ __forceinline__
template <int N> using IC = std::integral_constant<int, N>;

constexpr u32 WS_STAT = 0;
constexpr u32 WS_CS = 16384;
constexpr u32 WS_UFLAG = 30720;
constexpr u32 WS_YFLAG = 31744;
constexpr u32 WS_HFLAG = 35840;
constexpr u32 WS_R = 40960;
constexpr u32 WS_A = 45056;
constexpr u32 WS_N = 47104;
constexpr size_t WS_U = 1u << 20, WS_Y = 4u << 20, WS_H = 8u << 20, WS_DBG = 32u << 20;
constexpr u32 LA_XA = 16384, LA_WA = 17408, LA_RS = 18432, LA_ZERO = 18944;
constexpr u32 L_TAB = 0;
constexpr u32 L_KCP = 32768;
constexpr u32 L_UL = 66048;
constexpr u32 L_LNS = 66048;
constexpr u32 L_ST = 75264;
constexpr u32 L_RED = 0;
constexpr u32 LSPIN_MAX = 1u << 20;
constexpr u32 SPIN_MAX = 1u << 15;
constexpr u32 EP = 1u;
constexpr int LEADER = 248;

template <typename T> DEVI T ldsld(u32 a) { return *(AS3 T*)(uintptr_t)a; }
template <typename T> DEVI T ldsldv(u32 a) { return *(volatile AS3 T*)(uintptr_t)a; }
template <typename T> DEVI void ldsst(u32 a, T v) { *(AS3 T*)(uintptr_t)a = v; }
DEVI u32x2 ldstr(u32 a) { return __builtin_bit_cast(u32x2, __builtin_amdgcn_ds_read_tr16_b64_v4i16((AS3 s16x4*)(uintptr_t)a)); }
DEVI float bf2f(u32 v) { return __uint_as_float(v << 16); }
DEVI float bfhi(u32 v) { return __uint_as_float(v & 0xffff0000u); }
DEVI float rbf(float f) { return (float)(__bf16)f; }
DEVI u32 pkbf(float a, float b) { bf16x2 v = {(__bf16)a, (__bf16)b}; return __builtin_bit_cast(u32, v); }
DEVI u32 pkh(float a, float b) { f16x2 v = {(_Float16)a, (_Float16)b}; return __builtin_bit_cast(u32, v); }

template <int STEP, bool COH>
DEVI void glds2(const void* sbase, u32 v0, u32 v1, u32 lds0) {
  u32 keep;
  if (COH)
    asm volatile("s_mov_b32 %0, m0\n\ts_mov_b32 m0, %4\n\ts_nop 0\n\tglobal_load_lds_dwordx4 %1, %3 sc1\n\t"
                 "s_add_u32 m0, m0, %5\n\ts_nop 0\n\tglobal_load_lds_dwordx4 %2, %3 sc1\n\ts_mov_b32 m0, %0"
                 : "=&s"(keep) : "v"(v0), "v"(v1), "s"(sbase), "s"(lds0), "n"(STEP) : "memory", "scc");
  else
    asm volatile("s_mov_b32 %0, m0\n\ts_mov_b32 m0, %4\n\ts_nop 0\n\tglobal_load_lds_dwordx4 %1, %3\n\t"
                 "s_add_u32 m0, m0, %5\n\ts_nop 0\n\tglobal_load_lds_dwordx4 %2, %3\n\ts_mov_b32 m0, %0"
                 : "=&s"(keep) : "v"(v0), "v"(v1), "s"(sbase), "s"(lds0), "n"(STEP) : "memory", "scc");
}
template <int SSTEP, bool COH>
DEVI void glds8(const char* sbase, u32 v0, u32 v1, u32 lds0) {
#pragma unroll
  for (int j = 0; j < 4; j++) glds2<1024, COH>(sbase + (size_t)j * SSTEP, v0, v1, lds0 + j * 2048);
}
DEVI u32x4 mksrd(const void* p, u32 bytes) {
  u64 a = (u64)p;
  return (u32x4){(u32)a, (u32)(a >> 32) & 0xffffu, bytes, 0x00020000u};
}
template <bool COHA>
DEVI void dma4(u32x4 sA, u32 soA, u32 vA0, u32 vA1, u32x4 sB, u32 soB, u32 vB0, u32 vB1, u32 ldsA) {
  if (COHA)
    asm volatile("s_mov_b32 m0, %8\n\ts_nop 0\n\tbuffer_load_dwordx4 %2, %0, %1 offen sc1 lds\n\t"
                 "buffer_load_dwordx4 %3, %0, %1 offen offset:1024 sc1 lds\n\ts_add_u32 m0, m0, 0x2000\n\ts_nop 0\n\t"
                 "buffer_load_dwordx4 %6, %4, %5 offen lds\n\tbuffer_load_dwordx4 %7, %4, %5 offen offset:1024 lds"
                 ::"s"(sA), "s"(soA), "v"(vA0), "v"(vA1), "s"(sB), "s"(soB), "v"(vB0), "v"(vB1), "s"(ldsA) : "memory", "scc");
  else
    asm volatile("s_mov_b32 m0, %8\n\ts_nop 0\n\tbuffer_load_dwordx4 %2, %0, %1 offen lds\n\t"
                 "buffer_load_dwordx4 %3, %0, %1 offen offset:1024 lds\n\ts_add_u32 m0, m0, 0x2000\n\ts_nop 0\n\t"
                 "buffer_load_dwordx4 %6, %4, %5 offen lds\n\tbuffer_load_dwordx4 %7, %4, %5 offen offset:1024 lds"
                 ::"s"(sA), "s"(soA), "v"(vA0), "v"(vA1), "s"(sB), "s"(soB), "v"(vB0), "v"(vB1), "s"(ldsA) : "memory", "scc");
}
template <bool COH>
DEVI void dma2(u32x4 sA, u32 soA, u32 vA0, u32 vA1, u32 ldsA) {
  if (COH)
    asm volatile("s_mov_b32 m0, %4\n\ts_nop 0\n\tbuffer_load_dwordx4 %2, %0, %1 offen sc1 lds\n\t"
                 "buffer_load_dwordx4 %3, %0, %1 offen offset:1024 sc1 lds" ::"s"(sA), "s"(soA), "v"(vA0), "v"(vA1), "s"(ldsA) : "memory");
  else
    asm volatile("s_mov_b32 m0, %4\n\ts_nop 0\n\tbuffer_load_dwordx4 %2, %0, %1 offen lds\n\t"
                 "buffer_load_dwordx4 %3, %0, %1 offen offset:1024 lds" ::"s"(sA), "s"(soA), "v"(vA0), "v"(vA1), "s"(ldsA) : "memory");
}
template <bool COHA>
DEVI void dma11(u32x4 sA, u32 soA, u32 vA, u32x4 sB, u32 soB, u32 vB, u32 ldsA) {
  if (COHA)
    asm volatile("s_mov_b32 m0, %6\n\ts_nop 0\n\tbuffer_load_dwordx4 %2, %0, %1 offen sc1 lds\n\ts_add_u32 m0, m0, 0x2000\n\ts_nop 0\n\t"
                 "buffer_load_dwordx4 %5, %3, %4 offen lds" ::"s"(sA), "s"(soA), "v"(vA), "s"(sB), "s"(soB), "v"(vB), "s"(ldsA) : "memory", "scc");
  else
    asm volatile("s_mov_b32 m0, %6\n\ts_nop 0\n\tbuffer_load_dwordx4 %2, %0, %1 offen lds\n\ts_add_u32 m0, m0, 0x2000\n\ts_nop 0\n\t"
                 "buffer_load_dwordx4 %5, %3, %4 offen lds" ::"s"(sA), "s"(soA), "v"(vA), "s"(sB), "s"(soB), "v"(vB), "s"(ldsA) : "memory", "scc");
}
template <bool COH>
DEVI void dma1(u32x4 sA, u32 soA, u32 vA, u32 ldsA) {
  if (COH) asm volatile("s_mov_b32 m0, %3\n\ts_nop 0\n\tbuffer_load_dwordx4 %2, %0, %1 offen sc1 lds" ::"s"(sA), "s"(soA), "v"(vA), "s"(ldsA) : "memory");
  else asm volatile("s_mov_b32 m0, %3\n\ts_nop 0\n\tbuffer_load_dwordx4 %2, %0, %1 offen lds" ::"s"(sA), "s"(soA), "v"(vA), "s"(ldsA) : "memory");
}
template <bool COHA>
DEVI void dma21(u32x4 sA, u32 soA, u32 vA0, u32 vA1, u32x4 sB, u32 soB, u32 vB, u32 ldsA, u32 ldsB) {
  if (COHA)
    asm volatile("s_mov_b32 m0, %7\n\ts_nop 0\n\tbuffer_load_dwordx4 %2, %0, %1 offen sc1 lds\n\t"
                 "buffer_load_dwordx4 %3, %0, %1 offen offset:1024 sc1 lds\n\ts_mov_b32 m0, %8\n\ts_nop 0\n\tbuffer_load_dwordx4 %6, %4, %5 offen lds"
                 ::"s"(sA), "s"(soA), "v"(vA0), "v"(vA1), "s"(sB), "s"(soB), "v"(vB), "s"(ldsA), "s"(ldsB) : "memory");
  else
    asm volatile("s_mov_b32 m0, %7\n\ts_nop 0\n\tbuffer_load_dwordx4 %2, %0, %1 offen lds\n\t"
                 "buffer_load_dwordx4 %3, %0, %1 offen offset:1024 lds\n\ts_mov_b32 m0, %8\n\ts_nop 0\n\tbuffer_load_dwordx4 %6, %4, %5 offen lds"
                 ::"s"(sA), "s"(soA), "v"(vA0), "v"(vA1), "s"(sB), "s"(soB), "v"(vB), "s"(ldsA), "s"(ldsB) : "memory");
}
DEVI void rawbar() { asm volatile("" ::: "memory"); __builtin_amdgcn_s_barrier(); asm volatile("" ::: "memory"); }
DEVI void st16c(void* sbase, u32 voff, u32x4 v) {
  asm volatile("global_store_dwordx4 %0, %1, %2 sc1\n\ts_nop 1" ::"v"(voff), "v"(v), "s"(sbase) : "memory");
}
DEVI void st16(void* sbase, u32 voff, u32x4 v) {
  asm volatile("global_store_dwordx4 %0, %1, %2\n\ts_nop 1" ::"v"(voff), "v"(v), "s"(sbase) : "memory");
}
DEVI void st8c(void* sbase, u32 voff, u32x2 v) {
  asm volatile("global_store_dwordx2 %0, %1, %2 sc1\n\ts_nop 1" ::"v"(voff), "v"(v), "s"(sbase) : "memory");
}
DEVI void st4c(void* sbase, u32 voff, u32 v) {
  asm volatile("global_store_dword %0, %1, %2 sc1\n\ts_nop 1" ::"v"(voff), "v"(v), "s"(sbase) : "memory");
}
DEVI u32 ld4c_wait(const void* sbase, u32 voff) {
  u32 r;
  asm volatile("global_load_dword %0, %1, %2 sc1\n\ts_waitcnt vmcnt(0)" : "=&v"(r) : "v"(voff), "s"(sbase) : "memory");
  return r;
}
DEVI u32x4 ld16c_wait(const void* sbase, u32 voff) {
  u32x4 r;
  asm volatile("global_load_dwordx4 %0, %1, %2 sc1\n\ts_waitcnt vmcnt(0)" : "=&v"(r) : "v"(voff), "s"(sbase) : "memory");
  return r;
}
template <int N> DEVI void waitv() { asm volatile("s_waitcnt vmcnt(%0)" ::"n"(N) : "memory"); }
DEVI void bar() {
  asm volatile("s_waitcnt lgkmcnt(0)" ::: "memory");
  __builtin_amdgcn_s_barrier();
  asm volatile("" ::: "memory");
}
template <int CTRL, int RM> DEVI float dppf(float old, float v) {
  return __int_as_float(__builtin_amdgcn_update_dpp(__float_as_int(old), __float_as_int(v), CTRL, RM, 0xf, false));
}
DEVI float wave_sum(float v) {
  v += dppf<0x111, 0xf>(0.f, v); v += dppf<0x112, 0xf>(0.f, v); v += dppf<0x114, 0xf>(0.f, v); v += dppf<0x118, 0xf>(0.f, v);
  v += dppf<0x142, 0xa>(0.f, v); v += dppf<0x143, 0xc>(0.f, v);
  return __int_as_float(__builtin_amdgcn_readlane(__float_as_int(v), 63));
}
DEVI float wave_max0(float v) {
  v = fmaxf(v, dppf<0x111, 0xf>(0.f, v)); v = fmaxf(v, dppf<0x112, 0xf>(0.f, v)); v = fmaxf(v, dppf<0x114, 0xf>(0.f, v));
  v = fmaxf(v, dppf<0x118, 0xf>(0.f, v)); v = fmaxf(v, dppf<0x142, 0xa>(0.f, v)); v = fmaxf(v, dppf<0x143, 0xc>(0.f, v));
  return __int_as_float(__builtin_amdgcn_readlane(__float_as_int(v), 63));
}
#define CM_MUL(t, a, b) "v_pk_mul_f32 " t ", " a ", " b " op_sel_hi:[0,1]\n\t"
#define CM_FMA(r, a, b, t) "v_pk_fma_f32 " r ", " a ", " b ", " t " op_sel:[1,1,0] op_sel_hi:[1,0,1] neg_lo:[1,0,0]\n\t"
DEVI f32x2 cmul(f32x2 a, f32x2 b) {
  f32x2 t, r;
  asm(CM_MUL("%0", "%1", "%2") : "=v"(t) : "v"(a), "v"(b));
  asm(CM_FMA("%0", "%1", "%2", "%3") : "=v"(r) : "v"(a), "v"(b), "v"(t));
  return r;
}
DEVI void cmul4(f32x2& a0, f32x2& a1, f32x2& a2, f32x2& a3, f32x2 z) {
  f32x2 t0, t1, t2, t3;
  asm(CM_MUL("%0", "%4", "%8") CM_MUL("%1", "%5", "%8") CM_MUL("%2", "%6", "%8") CM_MUL("%3", "%7", "%8")
      CM_FMA("%0", "%4", "%8", "%0") CM_FMA("%1", "%5", "%8", "%1") CM_FMA("%2", "%6", "%8", "%2") CM_FMA("%3", "%7", "%8", "%3")
      : "=&v"(t0), "=&v"(t1), "=&v"(t2), "=&v"(t3) : "v"(a0), "v"(a1), "v"(a2), "v"(a3), "v"(z));
  a0 = t0; a1 = t1; a2 = t2; a3 = t3;
}
DEVI f32x2 cpow(float a2, float fhi, float flo, float R) {
  float m = __builtin_amdgcn_exp2f(a2 * R);
  float ph = __builtin_fmaf(flo, R, __builtin_amdgcn_fractf(fhi * R));
  return (f32x2){m * __builtin_amdgcn_cosf(ph), m * __builtin_amdgcn_sinf(ph)};
}
DEVI void pow2scale(float mx, int target, float& s, float& inv) {
  int ex = (int)((__float_as_uint(mx) >> 23) & 0xff);
  int se = 254 + target - ex;
  se = se < 2 ? 2 : (se > 252 ? 252 : se);
  s = __uint_as_float((u32)se << 23);
  inv = __uint_as_float((u32)(254 - se) << 23);
}

DEVI void sig(u32 a, u32 v) {
  asm volatile("s_mov_b64 exec, 1\n\tds_write_b32 %0, %1\n\ts_mov_b64 exec, -1" ::"v"(a), "v"(v) : "memory");
}
DEVI u32 min4(u32x4 f) { return __builtin_amdgcn_readfirstlane(min(min(f[0], f[1]), min(f[2], f[3]))); }
DEVI void wait4(u32 a, u32 v) {
  u32 spins = 0;
  while (min4(ldsldv<u32x4>(a)) < v && ++spins < LSPIN_MAX) __builtin_amdgcn_s_sleep(1);
  asm volatile("" ::: "memory");
}
DEVI u32 min2(u32x2 f) { return __builtin_amdgcn_readfirstlane(min(f[0], f[1])); }
DEVI void wait2(u32 a, u32 v) {
  u32 spins = 0;
  while (min2(ldsldv<u32x2>(a)) < v && ++spins < LSPIN_MAX) {}
  asm volatile("" ::: "memory");
}
DEVI void wait4f(u32 a, u32 v) {
  u32 spins = 0;
  while (min4(ldsldv<u32x4>(a)) < v && ++spins < LSPIN_MAX) {}
  asm volatile("" ::: "memory");
}
DEVI void wait8(u32 a, u32 v) {
  u32 spins = 0;
  for (;;) {
    u32x4 f0 = ldsldv<u32x4>(a), f1 = ldsldv<u32x4>(a + 16);
    if (min(min4(f0), min4(f1)) >= v || ++spins > LSPIN_MAX) break;
  }
  asm volatile("" ::: "memory");
}
DEVI void wait1(u32 a, u32 v) {
  u32 spins = 0;
  while (__builtin_amdgcn_readfirstlane(ldsldv<u32>(a)) < v && ++spins < LSPIN_MAX) __builtin_amdgcn_s_sleep(1);
  asm volatile("" ::: "memory");
}

__global__ __launch_bounds__(512, 4) void fused_kernel(
    const u16* __restrict__ x, const u16* __restrict__ Are, const u16* __restrict__ Aim, const u16* __restrict__ Bre,
    const u16* __restrict__ Bim, const u16* __restrict__ Cre, const u16* __restrict__ Cim, const u16* __restrict__ ldt,
    const u16* __restrict__ Dsk, const u16* __restrict__ w1, const u16* __restrict__ b1, const u16* __restrict__ w2,
    u16* __restrict__ out, char* __restrict__ ws) {
  __shared__ __attribute__((aligned(16))) char sm[81920];
  asm volatile("" ::"v"(sm) : "memory");
  const int tid = threadIdx.x, lane = tid & 63;
  const int w = __builtin_amdgcn_readfirstlane(tid >> 6);
  const int gb = blockIdx.x, b = gb & 255, xcd = b & 7, bi = b >> 3;
  const bool isg1 = bi < 28;
  const int bm = 2 * xcd + (bi & 1), bn = bi >> 1;
  const int tb = 32 * xcd + bi;
#define STAMP(i)
#define STAMP_OUT()
#define SB0 __builtin_amdgcn_sched_barrier(0)
  STAMP(0);
  const u32 ao = lane == 0 ? WS_N : WS_A + (u32)gb * 4;
#define SHK_POLL() asm volatile("global_load_dword v127, %0, %1 sc1" ::"v"(ao), "s"(ws) : "memory", "v127")
  u32 rb_ = 0, nsent = 0;
  bool rel = false;
  auto shk_first = [&]() {
    u32 n, a;
    asm volatile("v_readlane_b32 %0, v127, 0\n\tv_readlane_b32 %1, v127, 1" : "=s"(n), "=s"(a)::"memory");
    rb_ = a + 1u; nsent = n;
    if (lane == 0) st8c(ws, WS_R + (u32)gb * 8, (u32x2){rb_, n});
    SHK_POLL();
  };
  auto shk_step = [&]() {
    u32 n, a;
    asm volatile("v_readlane_b32 %0, v127, 0\n\tv_readlane_b32 %1, v127, 1" : "=s"(n), "=s"(a)::"memory");
    if (a == rb_) rel = true;
    else if (n != nsent) { nsent = n; if (lane == 0) st8c(ws, WS_R + (u32)gb * 8, (u32x2){rb_, n}); }
    SHK_POLL();
  };
  auto shk_finish = [&]() {
    u32 spins = 0;
    while (!rel && ++spins < SPIN_MAX) { waitv<0>(); shk_step(); }
    waitv<0>();
  };
  if (gb < 256) {
    const int bm2 = 4 * xcd + (bi & 3), bn2 = bi >> 2;
    const int ci = (7 - xcd) * 4 + (bi - 28);
    if (w < 4) {
      __builtin_amdgcn_s_setprio(3);
      const int hh = lane >> 5, blk = (lane >> 4) & 1, q4 = (lane & 15) >> 2, p4 = lane & 3;
      u32 sb = 0;
      if (isg1) {
        const int wf = w & 1, wt = w >> 1;
        const int tl = wt * 64 + (lane & 31);
        const u32 xa0 = (u32)(tl * 64 + ((hh ^ ((tl >> 2) & 3)) << 4));
        const u32 wa0 = (u32)(8192 + (8 * hh + q4) * 256 +
                              (((8 * wf + 4 * (p4 & 1) + blk) ^ (((q4 & 1) << 1) | ((q4 & 2) << 2))) << 4) + 8 * (p4 >> 1));
        f32x16 acc[2][2];
#pragma unroll
        for (int i = 0; i < 16; i++) { acc[0][0][i] = 0.f; acc[0][1][i] = 0.f; acc[1][0][i] = 0.f; acc[1][1][i] = 0.f; }
        bf16x8 FX[3][2], FW[3][2];
        auto half = [&](auto C_, auto R_, auto KS_) {
          constexpr int C = decltype(C_)::value, R = decltype(R_)::value, ks = decltype(KS_)::value;
          const u32 ax = sb + (xa0 ^ (u32)(ks << 5)), aw0 = sb + wa0 + ks * 4096, aw1 = sb + (wa0 ^ 32u) + ks * 4096;
          u32x2 a0, a1, a2, a3;
          SB0; acc[0][0] = __builtin_amdgcn_mfma_f32_32x32x16_bf16(FW[C][0], FX[C][0], acc[0][0], 0, 0, 0); SB0;
          FX[R][0] = ldsld<bf16x8>(ax); a0 = ldstr(aw0);
          SB0; acc[0][1] = __builtin_amdgcn_mfma_f32_32x32x16_bf16(FW[C][0], FX[C][1], acc[0][1], 0, 0, 0); SB0;
          a1 = ldstr(aw0 + 1024); FX[R][1] = ldsld<bf16x8>(ax + 2048);
          SB0; acc[1][0] = __builtin_amdgcn_mfma_f32_32x32x16_bf16(FW[C][1], FX[C][0], acc[1][0], 0, 0, 0); SB0;
          a2 = ldstr(aw1); a3 = ldstr(aw1 + 1024);
          SB0; acc[1][1] = __builtin_amdgcn_mfma_f32_32x32x16_bf16(FW[C][1], FX[C][1], acc[1][1], 0, 0, 0); SB0;
          FW[R][0] = __builtin_bit_cast(bf16x8, (u32x4){a0[0], a0[1], a1[0], a1[1]});
          FW[R][1] = __builtin_bit_cast(bf16x8, (u32x4){a2[0], a2[1], a3[0], a3[1]});
        };
        auto step = [&](auto A_, auto B_, auto C_) {
          rawbar();
          sb = sb == 65536u ? 0u : sb + 16384u;
          half(A_, C_, IC<0>{}); half(B_, A_, IC<1>{});
        };
        rawbar();
        STAMP(1);
#pragma unroll
        for (int ks = 0; ks < 2; ks++)
#pragma unroll
          for (int j = 0; j < 2; j++) {
            FX[ks][j] = ldsld<bf16x8>((xa0 ^ (u32)(ks << 5)) + j * 2048);
            u32 a = (wa0 ^ (u32)(j << 5)) + ks * 4096;
            u32x2 a0 = ldstr(a), a1 = ldstr(a + 1024);
            FW[ks][j] = __builtin_bit_cast(bf16x8, (u32x4){a0[0], a0[1], a1[0], a1[1]});
          }
#pragma unroll 1
        for (int s = 0; s < 15; s += 3) { step(IC<0>{}, IC<1>{}, IC<2>{}); step(IC<2>{}, IC<0>{}, IC<1>{}); step(IC<1>{}, IC<2>{}, IC<0>{}); }
#pragma unroll
        for (int ks = 0; ks < 2; ks++)
#pragma unroll
          for (int tau = 0; tau < 2; tau++)
#pragma unroll
            for (int j = 0; j < 2; j++)
              acc[tau][j] = __builtin_amdgcn_mfma_f32_32x32x16_bf16(FW[ks][tau], FX[ks][j], acc[tau][j], 0, 0, 0);
        STAMP(2);
        bar();
        STAMP(3);
        int l2 = lane;
        asm volatile("" : "+v"(l2));
        const int hh2 = l2 >> 5, tl2 = wt * 64 + (l2 & 31);
        {
          const int li = l2 & 15;
          const u32 fo = (u32)(wf * 64 + 32 * ((li >> 2) & 1) + 8 * ((l2 >> 4) & 1) + 4 * (li >> 3) + (li & 3));
          bf16x8 xa[2], wa[2];
#pragma unroll
          for (int j = 0; j < 2; j++) { u32x2 t = ldsld<u32x2>(hh2 ? LA_ZERO : LA_XA + (tl2 + 32 * j) * 8); xa[j] = __builtin_bit_cast(bf16x8, (u32x4){t[0], t[1], 0u, 0u}); }
#pragma unroll
          for (int tau = 0; tau < 2; tau++) { u32x2 t = ldsld<u32x2>(hh2 ? LA_ZERO : LA_WA + (fo + 16 * tau) * 8); wa[tau] = __builtin_bit_cast(bf16x8, (u32x4){t[0], t[1], 0u, 0u}); }
#pragma unroll
          for (int tau = 0; tau < 2; tau++)
#pragma unroll
            for (int j = 0; j < 2; j++) acc[tau][j] = __builtin_amdgcn_mfma_f32_32x32x16_bf16(wa[tau], xa[j], acc[tau][j], 0, 0, 0);
        }
        char* hp = ws + WS_H + (size_t)(bm * 2 + wt) * 229376 + (bn * 16 + wf * 8) * 1024;
#pragma unroll
        for (int j = 0; j < 2; j++) {
          const float r = ldsld<float>(LA_RS + (tl2 + 32 * j) * 4);
          const float c1 = r * 1.44269504088896341f, m2r = -2.f * r;
          const f32x2 c1v = {c1, c1}, rv = {r, r}, m2v = {m2r, m2r}, one = {1.f, 1.f};
#pragma unroll
          for (int tau = 0; tau < 2; tau++) {
#pragma unroll
            for (int hf = 0; hf < 2; hf++) {
              f32x2 av[4], t[4];
#pragma unroll
              for (int jj = 0; jj < 4; jj++) { av[jj] = (f32x2){acc[tau][j][8 * hf + 2 * jj], acc[tau][j][8 * hf + 2 * jj + 1]}; t[jj] = av[jj] * c1v; }
#pragma unroll
              for (int jj = 0; jj < 4; jj++) t[jj] = (f32x2){__builtin_amdgcn_exp2f(t[jj][0]), __builtin_amdgcn_exp2f(t[jj][1])};
#pragma unroll
              for (int jj = 0; jj < 4; jj++) t[jj] = t[jj] + one;
#pragma unroll
              for (int jj = 0; jj < 4; jj++) t[jj] = __builtin_elementwise_fma(t[jj], t[jj], one);
#pragma unroll
              for (int jj = 0; jj < 4; jj++) t[jj] = (f32x2){__builtin_amdgcn_rcpf(t[jj][0]), __builtin_amdgcn_rcpf(t[jj][1])};
#pragma unroll
              for (int jj = 0; jj < 4; jj++) t[jj] = __builtin_elementwise_fma(t[jj], m2v, rv);
              u32x4 o;
#pragma unroll
              for (int jj = 0; jj < 4; jj++) { f32x2 ov = av[jj] * t[jj]; o[jj] = pkbf(ov[0], ov[1]); }
              st16c(hp, (u32)((4 * hh2 + 2 * tau + hf) * 1024 + ((l2 & 31) + 32 * j) * 16), o);
            }
          }
        }
        STAMP(4);
        waitv<0>();
        if (l2 == 0) st4c(ws, WS_HFLAG + (u32)((bm * 2 + wt) * 32 + bn * 2 + wf) * 4, EP);
        STAMP(5);
        bar();
      } else {
        const bool lead = b == LEADER && w == 1;
        u32 cand = 0;
        u32x4 r0, r1, r2, r3;
#define LEAD_LOAD() asm volatile("global_load_dwordx4 %0, %4, %5 sc1\n\tglobal_load_dwordx4 %1, %4, %5 offset:16 sc1\n\t" \
                       "global_load_dwordx4 %2, %4, %5 offset:32 sc1\n\tglobal_load_dwordx4 %3, %4, %5 offset:48 sc1\n\ts_waitcnt vmcnt(0)" \
                       : "=&v"(r0), "=&v"(r1), "=&v"(r2), "=&v"(r3) : "v"((u32)lane * 64), "s"(ws + WS_R) : "memory")
        if (lead) {
          asm volatile("global_load_dword %0, %1, %2 sc1" : "=&v"(cand) : "v"(WS_N), "s"(ws) : "memory");
          LEAD_LOAD();
          asm volatile("" : "+v"(cand)::"memory");
          cand += 1u;
          while (__any(r0[1] == cand || r0[3] == cand || r1[1] == cand || r1[3] == cand || r2[1] == cand || r2[3] == cand ||
                       r3[1] == cand || r3[3] == cand)) cand++;
          if (lane == 0) st4c(ws, WS_N, cand);
          STAMP(10);
        }
        rawbar();
        if (ci < 28) {
          float cs[8];
#pragma unroll
          for (int e = 0; e < 8; e++) cs[e] = 0.f;
#pragma unroll 4
          for (int j = 0; j < 16; j++) {
            u32x4 cw = ldsld<u32x4>((u32)(j * 4096 + tid * 16));
#pragma unroll
            for (int e = 0; e < 4; e++) { cs[2 * e] += bf2f(cw[e]); cs[2 * e + 1] += bfhi(cw[e]); }
          }
          ldsst<f32x4>((u32)(65536 + tid * 32), (f32x4){cs[0], cs[1], cs[2], cs[3]});
          ldsst<f32x4>((u32)(65536 + tid * 32 + 16), (f32x4){cs[4], cs[5], cs[6], cs[7]});
        }
        bar();
        if (ci < 28 && w == 0) {
          float s = 0.f;
#pragma unroll 8
          for (int r = 0; r < 32; r++) s += ldsld<float>((u32)(65536 + r * 256 + lane * 4));
          st8c(ws, WS_CS + (u32)(ci * 512 + lane * 8), (u32x2){__float_as_uint(s), EP});
        }
        if (lead) {
          u32 spins = 0;
          for (;;) {
            LEAD_LOAD();
            bool ok = r0[1] == cand && r0[3] == cand && r1[1] == cand && r1[3] == cand && r2[1] == cand && r2[3] == cand &&
                      r3[1] == cand && r3[3] == cand;
            if (__all(ok) || ++spins > SPIN_MAX) break;
          }
          st16c(ws + WS_A, (u32)lane * 32, (u32x4){r0[0], r0[2], r1[0], r1[2]});
          st16c(ws + WS_A, (u32)lane * 32 + 16, (u32x4){r2[0], r2[2], r3[0], r3[2]});
          STAMP(11);
        }
      }
      int l3 = lane;
      asm volatile("" : "+v"(l3));
      const int h3 = l3 >> 5, blk3 = (l3 >> 4) & 1, q3 = (l3 & 15) >> 2, p3 = l3 & 3;
      sb = 16384u;
      const int wd = w & 1, wt2 = w >> 1;
      const int tl = wt2 * 32 + (l3 & 31);
      const u32 ha0 = (u32)(h3 * 1024 + tl * 16);
      const u32 wa0 = (u32)(8192 + (8 * h3 + q3) * 128 + (((4 * wd + 2 * (p3 & 1) + blk3) ^ ((q3 & 2) << 1)) << 4) + 8 * (p3 >> 1));
      const int dl = wd * 32 + 16 * h3;
      f32x16 acc;
#pragma unroll
      for (int i = 0; i < 16; i++) acc[i] = 0.f;
      bf16x8 FH[2][4], FW[2][4];
      auto rdf2 = [&](auto P_, int ks) {
        constexpr int P = decltype(P_)::value;
        FH[P][ks] = ldsld<bf16x8>(sb + ha0 + ks * 2048);
        u32 a = sb + wa0 + ks * 2048;
        u32x2 a0 = ldstr(a), a1 = ldstr(a + 512);
        FW[P][ks] = __builtin_bit_cast(bf16x8, (u32x4){a0[0], a0[1], a1[0], a1[1]});
      };
      auto step2 = [&](auto P_) {
        constexpr int P = decltype(P_)::value;
        rawbar();
        sb = sb == 65536u ? 0u : sb + 16384u;
#pragma unroll
        for (int ks = 0; ks < 4; ks++) {
          SB0; acc = __builtin_amdgcn_mfma_f32_32x32x16_bf16(FW[P][ks], FH[P][ks], acc, 0, 0, 0); SB0;
          rdf2(IC<1 - P>{}, ks);
        }
      };
      bar();
      STAMP(6);
      rawbar();
      STAMP(7);
#pragma unroll
      for (int ks = 0; ks < 4; ks++) rdf2(IC<0>{}, ks);
#pragma unroll 1
      for (int s = 0; s < 26; s += 2) { step2(IC<0>{}); step2(IC<1>{}); }
      step2(IC<0>{});
#pragma unroll
      for (int ks = 0; ks < 4; ks++) acc = __builtin_amdgcn_mfma_f32_32x32x16_bf16(FW[1][ks], FH[1][ks], acc, 0, 0, 0);
      STAMP(8);
      rawbar();
      const u32 LA_YT = 65536u, LA_OUT = 73728u;
      u32 ya = LA_YT + (u32)((dl + q3) * 128 + (p3 >> 1) * 64 + (wt2 * 2 + blk3) * 16 + (p3 & 1) * 8);
#pragma unroll
      for (int hf = 0; hf < 2; hf++) {
        u32x2 y0 = ldstr(ya + hf * 1024), y1 = ldstr(ya + hf * 1024 + 512);
        u32x4 yy = {y0[0], y0[1], y1[0], y1[1]};
        const u32 oa = LA_OUT + (u32)(tl * 128 + (((4 * wd + 2 * h3 + hf) ^ (tl & 7)) << 4));
        u32x4 xv = ldsld<u32x4>(oa);
        u32x4 ov;
#pragma unroll
        for (int j = 0; j < 4; j++) {
          u32 s1 = pkbf(bf2f(xv[j]) + bf2f(yy[j]), bfhi(xv[j]) + bfhi(yy[j]));
          u32 s2 = pkbf(acc[8 * hf + 2 * j], acc[8 * hf + 2 * j + 1]);
          ov[j] = pkbf(bf2f(s1) + bf2f(s2), bfhi(s1) + bfhi(s2));
        }
        ldsst<u32x4>(oa, ov);
      }
#pragma unroll
      for (int j = 0; j < 2; j++) {
        int t = wt2 * 32 + (l3 >> 2) + 16 * j, c = 4 * wd + (l3 & 3);
        u32x4 ov = ldsld<u32x4>(LA_OUT + (u32)(t * 128 + ((c ^ (t & 7)) << 4)));
        st16((char*)out + (size_t)bm2 * 65536 + bn2 * 128, (u32)(t * 1024 + c * 16), ov);
      }
      STAMP(9);
    } else {
      __builtin_amdgcn_s_setprio(2);
      const int lw = w - 4;
      const bool shk = lw == 3;
      const u32 rot2 = (u32)((bi * 9) % 28), rot1 = (u32)(2 * ((2 * bm + bn) & 7));
      u32 v2, vX, vW;
      {
        const int row2 = 16 * lw + (lane >> 3);
        v2 = (u32)(row2 * 1024 + (((lane & 7) ^ ((row2 & 2) << 1)) << 4) + bn2 * 128);
        int row = 32 * lw + (lane >> 2);
        vX = (u32)((bm * 128 + row) * 1024 + (((lane & 3) ^ ((row >> 2) & 3)) << 4));
        int k = 8 * lw + (lane >> 4);
        vW = (u32)(k * 3584 + (((lane & 15) ^ (((k & 1) << 1) | ((k & 2) << 2))) << 4) + bn * 256);
      }
      const u32x4 sW2 = mksrd(w2, 1792u * 1024u), sX = mksrd(x, 2048u * 1024u), sW1 = mksrd(w1, 512u * 3584u);
      u32 so2 = rot2 * 65536u, soX = rot1 * 64u, soW = rot1 * 114688u, sl = (u32)(lw * 2048);
      const u32 slend = 81920u + (u32)(lw * 2048);
      auto nextsl = [&]() { sl += 16384u; sl = sl == slend ? sl - 81920u : sl; };
      auto next2 = [&]() { so2 += 65536u; so2 = so2 == 28u * 65536u ? 0u : so2; };
      auto i1 = [&]() {
        dma4<false>(sX, soX, vX, vX + 15360u, sW1, soW, vW, vW + 13312u, sl);
        soX = (soX + 64u) & 1023u;
        soW += 114688u; soW = soW == 16u * 114688u ? 0u : soW;
        nextsl();
      };
      auto iw2 = [&]() { dma2<false>(sW2, so2, v2, v2 + 7168u, sl + 8192u); next2(); nextsl(); };
      const char* sb_ = ws + WS_STAT + bm * 1024;
      const char* cb_ = ws + WS_CS + bn * 1024;
#define HLP_POLL() asm volatile("global_load_dwordx4 v[116:119], %0, %1 sc1\n\tglobal_load_dwordx4 v[120:123], %0, %2 sc1" \
                                ::"v"((u32)lane * 16), "s"(sb_), "s"(cb_) : "memory", "v116", "v117", "v118", "v119", "v120", "v121", "v122", "v123")
      if (isg1) {
        if (shk) {
          SHK_POLL();
          asm volatile("global_load_dword v124, %0, %1" ::"v"((u32)lane * 4), "s"(b1 + bn * 128) : "memory", "v124");
        }
#pragma unroll 1
        for (int q = 0; q < 5; q++) i1();
        waitv<16>(); rawbar();
        if (shk) {
          bool asked = false;
          shk_first();
          waitv<12>(); rawbar();
#pragma unroll 1
          for (int s = 1; s <= 14; s++) {
            if (s >= 13) waitv<4>(); else waitv<8>();
            rawbar();
            if (s <= 11) i1(); else iw2();
            if (!rel) { if (s & 1) shk_step(); }
            else if (!asked) { HLP_POLL(); asked = true; }
          }
          STAMP(1);
          shk_finish();
          STAMP(2);
          u32 spins = 0;
          for (;;) {
            if (!asked) HLP_POLL();
            asked = false;
            waitv<0>();
            u64 m0_, m1_, m2_, m3_;
            asm volatile("v_cmp_ne_u32 %0, 1, v117\n\tv_cmp_ne_u32 %1, 1, v119\n\tv_cmp_ne_u32 %2, 1, v121\n\tv_cmp_ne_u32 %3, 1, v123"
                         : "=&s"(m0_), "=&s"(m1_), "=&s"(m2_), "=&s"(m3_)::"memory");
            if ((m0_ | m1_ | m2_ | m3_) == 0 || ++spins > SPIN_MAX) break;
          }
          u32 g0, g1, c0, c1, bv;
          asm volatile("v_mov_b32 %0, v116\n\tv_mov_b32 %1, v118\n\tv_mov_b32 %2, v120\n\tv_mov_b32 %3, v122\n\tv_mov_b32 %4, v124"
                       : "=&v"(g0), "=&v"(g1), "=&v"(c0), "=&v"(c1), "=&v"(bv)::"memory");
          u32 n0 = (g0 & 0xffffu) ^ 0x8000u, n1 = (g1 & 0xffffu) ^ 0x8000u;
          ldsst<u32x4>(LA_XA + lane * 16, (u32x4){n0 | (n0 << 16), g0 >> 16, n1 | (n1 << 16), g1 >> 16});
          ldsst<f32x2>(LA_RS + lane * 8, (f32x2){1.f / bfhi(g0), 1.f / bfhi(g1)});
          float f0 = __uint_as_float(c0), f1 = __uint_as_float(c1), h0 = rbf(f0), h1 = rbf(f1);
          ldsst<u32x4>(LA_WA + lane * 16, (u32x4){pkbf(h0, f0 - h0), bv & 0xffffu, pkbf(h1, f1 - h1), bv >> 16});
          if (lane < 2) ldsst<u32>(LA_ZERO + lane * 4, 0u);
          STAMP(3);
        } else {
          waitv<12>(); rawbar();
#pragma unroll 1
          for (int s = 1; s <= 11; s++) { waitv<8>(); rawbar(); i1(); }
          waitv<8>(); rawbar(); iw2(); waitv<6>(); rawbar(); iw2(); waitv<4>(); rawbar(); iw2();
        }
        bar();
        iw2(); iw2();
        rawbar();
      } else {
        if (shk) {
          if (ci < 28) st4c(ws, WS_CS + (u32)(ci * 512 + lane * 8 + 4), 0u);
          SHK_POLL();
        }
        if (ci < 28) {
          const u32 vc = (u32)((16 * lw + (lane >> 3)) * 3584 + (lane & 7) * 16 + ci * 128);
#pragma unroll 1
          for (int j = 0; j < 8; j++) dma2<false>(sW1, (u32)j * 64u * 3584u, vc, vc + 8u * 3584u - 1024u, (u32)(j * 8192 + lw * 2048));
        }
        waitv<0>();
        rawbar();
        if (shk) shk_first();
        rawbar();
        sl += 16384u;
#pragma unroll 1
        for (int q = 0; q < 5; q++) iw2();
        if (shk) shk_finish();
      }
      if (shk) {
        u32 spins = 0;
        for (;;) {
          u32 f = ld4c_wait(ws + WS_HFLAG + bm2 * 128, (u32)(lane < 28 ? lane : 0) * 4);
          if (__all(f == EP) || ++spins > SPIN_MAX) break;
          __builtin_amdgcn_s_sleep(8);
        }
        STAMP(4);
      }
      const u32x4 sH = mksrd(ws + WS_H + (size_t)bm2 * 229376, 229376u);
      const u32 vH = (u32)(lw * 2048 + lane * 16);
      u32 soH = rot2 * 8192u;
      auto nextH = [&]() { soH += 8192u; soH = soH == 229376u ? 0u : soH; };
      auto i2 = [&]() { dma4<true>(sH, soH, vH, vH, sW2, so2, v2, v2 + 7168u, sl); nextH(); next2(); nextsl(); };
      rawbar();
#pragma unroll 1
      for (int q = 0; q < 5; q++) { dma2<true>(sH, soH, vH, vH, sl); nextH(); nextsl(); }
      waitv<8>(); rawbar();
      waitv<6>(); rawbar();
      waitv<4>(); rawbar(); i2();
      waitv<6>(); rawbar(); i2();
      const int ya_ = bm2 >> 2;
      const char* yfb = ws + WS_YFLAG + ((ya_ >> 2) * 512 + bn2 * 64) * 4;
#pragma unroll 1
      for (int s = 3; s < 18; s++) { waitv<8>(); rawbar(); i2(); }
      asm volatile("global_load_dword v126, %0, %1 sc1" ::"v"((u32)lane * 4), "s"(yfb) : "memory", "v126");
#pragma unroll 1
      for (int s = 18; s < 24; s++) { waitv<8>(); rawbar(); i2(); }
      waitv<8>(); rawbar();
      {
        u64 ne;
        asm volatile("v_cmp_ne_u32 %0, 1, v126" : "=s"(ne)::"memory");
        const u32x4 sY = mksrd(ws + WS_Y, 2048u * 1024u);
        const u32 vy = (u32)((bn2 * 64 + 16 * lw + (lane >> 3)) * 4096 + ya_ * 512 + (bm2 & 3) * 64 + ((lane >> 2) & 1) * 256 + (lane & 3) * 16);
        const u32 vx = (u32)((bm2 * 64 + 16 * lw + (lane >> 3)) * 1024 + bn2 * 128 + (((lane & 7) ^ (lane >> 3)) << 4));
        if (ne == 0) {
          dma4<true>(sY, 0u, vy, vy + 32768u - 1024u, sX, 0u, vx, vx + 7168u, 65536u + (u32)lw * 2048u);
          waitv<8>(); rawbar(); waitv<4>(); rawbar();
        } else {
          waitv<4>(); rawbar(); waitv<0>(); rawbar();
          u32 spins = 0, yf;
          do { yf = ld4c_wait(yfb, (u32)lane * 4); } while (!__all(yf == EP) && ++spins < SPIN_MAX);
          dma4<true>(sY, 0u, vy, vy + 32768u - 1024u, sX, 0u, vx, vx + 7168u, 65536u + (u32)lw * 2048u);
        }
      }
      waitv<0>();
      rawbar();
      STAMP(7);
    }
  } else {
    const int G = w >> 2, kch = (w >> 1) & 1, kmh = w & 1;
    const int dch = 2 * tb + kch;
    const bool shk = w == 7;
    __builtin_amdgcn_s_setprio(2);
    int ust = 0;
#define UG_POLL() asm volatile("global_load_dwordx4 v[120:123], %0, %1 sc1" ::"v"((u32)lane * 16), "s"(ws + WS_UFLAG) : "memory", "v120", "v121", "v122", "v123")
    auto bsvc = [&]() {
      if (ust == 0) {
        shk_step();
        if (rel) { asm volatile("v_mov_b32 v120, 0" ::: "v120"); UG_POLL(); ust = 1; }
      } else if (ust == 1) {
        u64 m0_, m1_, m2_, m3_;
        asm volatile("v_cmp_ne_u32 %0, 1, v120\n\tv_cmp_ne_u32 %1, 1, v121\n\tv_cmp_ne_u32 %2, 1, v122\n\tv_cmp_ne_u32 %3, 1, v123"
                     : "=&s"(m0_), "=&s"(m1_), "=&s"(m2_), "=&s"(m3_)::"memory");
        if ((m0_ | m1_ | m2_ | m3_) == 0) {
          u32 vo = (u32)lane * 8192;
#pragma unroll
          for (int j = 0; j < 4; j++)
            glds2<4608, true>(ws + WS_U + (size_t)tb * 32 + (size_t)j * 524288, vo, vo + 16, L_UL + 512 + (u32)j * 1024);
          ust = 2;
        } else UG_POLL();
      }
    };
    u32x4 xr;
    u32 pa, pb, pc_, pd, pe, pf, pl, pk_;
    {
      u32 vo = (u32)((8 * tb + w) * 1024 + lane * 16);
      u32 po = (u32)((dch * 128 + kmh * 64 + lane) * 2);
      u32 lo = (u32)(dch * 2);
      asm volatile(
          "global_load_dwordx4 %0, %9, %12\n\t"
          "global_load_ushort %1, %10, %13\n\t"
          "global_load_ushort %2, %10, %14\n\t"
          "global_load_ushort %3, %10, %15\n\t"
          "global_load_ushort %4, %10, %16\n\t"
          "global_load_ushort %5, %10, %17\n\t"
          "global_load_ushort %6, %10, %18\n\t"
          "global_load_ushort %7, %11, %19\n\t"
          "global_load_ushort %8, %11, %20"
          : "=&v"(xr), "=&v"(pa), "=&v"(pb), "=&v"(pc_), "=&v"(pd), "=&v"(pe), "=&v"(pf), "=&v"(pl), "=&v"(pk_)
          : "v"(vo), "v"(po), "v"(lo), "s"(x), "s"(Are), "s"(Aim), "s"(Bre), "s"(Bim), "s"(Cre), "s"(Cim), "s"(ldt), "s"(Dsk)
          : "memory");
    }
    if (shk) {
      const u32 os = WS_STAT + (u32)(8 * tb + lane) * 8 + 4;
      const u32 oy = WS_YFLAG + (u32)((lane & 1) * 512 + 2 * tb + ((lane >> 1) & 1)) * 4;
      const u32 oh = WS_HFLAG + (u32)((bm * 2 + ((lane >> 1) & 1)) * 32 + bn * 2 + (lane & 1)) * 4;
      u32 off = lane < 8 ? os : (lane < 12 ? oy : ((lane < 16 && isg1) ? oh : WS_UFLAG + (u32)tb * 4));
      if (lane < 17) st4c(ws, off, 0u);
      SHK_POLL();
    }
    const int ech = tid >> 8, eg = tid & 255;
    const u32 pb0 = L_TAB + (u32)ech * 16384;
    u32 ad[12];
    {
      const float lf = (float)(2047 - 8 * eg);
      const u32 cb0 = pb0 - 0x3f00u * 4u;
#pragma unroll
      for (int e = 0; e < 12; e += 2) {
        u32 pk2 = pkbf(fmaxf(lf - (float)e, 0.5f), fmaxf(lf - (float)(e + 1), 0.5f));
        ad[e] = cb0 + ((pk2 & 0xffffu) << 2);
        ad[e + 1] = cb0 + ((pk2 >> 16) << 2);
        asm volatile("" : "+v"(ad[e]), "+v"(ad[e + 1]));
      }
    }
    if (shk) waitv<2>(); else waitv<0>();
    asm volatile("" : "+v"(xr), "+v"(pa), "+v"(pb), "+v"(pc_), "+v"(pd), "+v"(pe), "+v"(pf), "+v"(pl), "+v"(pk_)::"memory");
    STAMP(1);
    {
      float v[8];
#pragma unroll
      for (int j = 0; j < 4; j++) { v[2 * j] = bf2f(xr[j]); v[2 * j + 1] = bfhi(xr[j]); }
      float mean = wave_sum(((v[0] + v[1]) + (v[2] + v[3])) + ((v[4] + v[5]) + (v[6] + v[7]))) * (1.f / 512.f);
      float q = 0.f;
#pragma unroll
      for (int j = 0; j < 8; j++) { float d = v[j] - mean; q = __builtin_fmaf(d, d, q); }
      float var = wave_sum(q) * (1.f / 512.f);
      float mb = rbf(mean), sd = rbf(__builtin_sqrtf(rbf(rbf(var) + 1e-5f)));
      float rs = __builtin_amdgcn_rcpf(sd);
      u32x4 o;
#pragma unroll
      for (int j = 0; j < 4; j++) {
        u32 dd = pkbf(v[2 * j] - mb, v[2 * j + 1] - mb);
        float d0 = bf2f(dd), d1 = bfhi(dd);
        float q0 = d0 * rs, q1 = d1 * rs;
        q0 = __builtin_fmaf(__builtin_fmaf(-q0, sd, d0), rs, q0);
        q1 = __builtin_fmaf(__builtin_fmaf(-q1, sd, d1), rs, q1);
        u32 qq = pkbf(q0, q1);
        o[j] = pkh(bf2f(qq), bfhi(qq));
      }
      ldsst<u32x4>(L_LNS + w * 1088 + lane * 16, o);
      if (lane == 0) ldsst<u32>(L_ST + w * 4, (__float_as_uint(mb) >> 16) | (__float_as_uint(sd) & 0xffff0000u));
    }
    if (shk) { waitv<0>(); shk_first(); }
    bar();
    if (shk) {
      const int li = lane & 15;
      u32 a = L_LNS + (li >> 2) * 1088 + ((lane & 48) + 4 * (li & 3)) * 2;
#pragma unroll
      for (int j = 0; j < 8; j++) {
        u32x2 p0 = ldstr(a + j * 128), p1 = ldstr(a + j * 128 + 4 * 1088);
        st16c(ws + WS_U + (size_t)tb * 8192, (u32)(j * 1024 + lane * 16), (u32x4){p0[0], p0[1], p1[0], p1[1]});
      }
      u32 pay = ldsld<u32>(L_ST + (lane & 7) * 4);
      if (lane < 8) st8c(ws, WS_STAT + (u32)(8 * tb + lane) * 8, (u32x2){pay, EP});
      bsvc();
    }
    STAMP(2);
    const int rr = lane & 15, kg = lane >> 4;
    {
      float dt = rbf(expf(bf2f(pl)));
      float a2 = (dt * bf2f(pa)) * 1.44269504088896341f;
      float bb = dt * bf2f(pb);
      f32x2 W = cmul((f32x2){bf2f(pe), bf2f(pf)}, (f32x2){dt * bf2f(pc_), dt * bf2f(pd)});
      const float C_HI = 0.15915494309189535f, C_LO = (float)(0.15915494309189533577 - (double)0.15915494309189535f);
      float p = bb * C_HI;
      float er = __builtin_fmaf(bb, C_HI, -p);
      float fhi = __uint_as_float(__float_as_uint(p) & 0xfffff000u);
      float flo = (p - fhi) + __builtin_fmaf(bb, C_LO, er);
      float g = 0.f;
      if (__any(a2 > 0.f)) { g = wave_max0(fmaxf(a2, 0.f)); a2 -= g; }
      float wsum = wave_sum(fabsf(W[0]) + fabsf(W[1]));
      float sig_, isig;
      pow2scale(wsum, 13, sig_, isig);
      const f32x2 W0 = {W[0] * sig_, -W[1] * sig_};
      const u32 T = L_TAB + w * 8192;
      const u32 wbase = T + (lane >> 2) * 16 + (lane & 3) * 4;
      const u32 ra = T + rr * 256 + ((kg ^ rr) << 4);
      const float cs = G ? 8.f : 2.f, rs = G ? 128.f : 32.f, rb = G ? 512.f : 0.f;
      f32x2 zc = cpow(a2, -fhi, -flo, cs), zr = cpow(a2, fhi, flo, rs);
      f32x2 zc4 = cpow(a2, -fhi, -flo, 4.f * cs), zr4 = cpow(a2, fhi, flo, 4.f * rs);
      f32x2 zb = cpow(a2, fhi, flo, rb), zx = cpow(a2, fhi, flo, 0.5f * cs);
      f32x2 zc2 = cmul(zc, zc), zr2 = cmul(zr, zr);
      f32x2 c[4], r[4], e0[4], e1[4];
      c[0] = W0; c[1] = cmul(W0, zc); r[0] = zb; r[1] = cmul(zb, zr);
      c[2] = cmul(c[0], zc2); c[3] = cmul(c[1], zc2); r[2] = cmul(r[0], zr2); r[3] = cmul(r[1], zr2);
      SB0;
      if (shk) { waitv<0>(); if (lane == 0) st4c(ws, WS_UFLAG + (u32)tb * 4, EP); bsvc(); }
      SB0;
#pragma unroll
      for (int o = 0; o < 4; o++) {
        if (o > 0) { cmul4(c[0], c[1], c[2], c[3], zc4); cmul4(r[0], r[1], r[2], r[3], zr4); }
        if (o == 0) { e0[0] = r[0]; e0[1] = r[1]; e0[2] = r[2]; e0[3] = r[3]; cmul4(e0[0], e0[1], e0[2], e0[3], zx); }
        if (o == 1) { e1[0] = r[0]; e1[1] = r[1]; e1[2] = r[2]; e1[3] = r[3]; cmul4(e1[0], e1[1], e1[2], e1[3], zx); }
#pragma unroll
        for (int j = 0; j < 4; j++) {
          int e = 4 * o + j;
          u32 a = wbase ^ (u32)(e << 4);
          ldsst<u32>(a + e * 256, pkh(r[j][0], r[j][1]));
          ldsst<u32>(a + e * 256 + 4096, pkh(c[j][0], c[j][1]));
        }
        if (shk) bsvc();
      }
      f16x8 af[4], bf[4];
#pragma unroll
      for (int s = 0; s < 4; s++) { af[s] = ldsld<f16x8>(ra ^ (u32)(s << 6)); bf[s] = ldsld<f16x8>((ra ^ (u32)(s << 6)) + 4096); }
#pragma unroll
      for (int j = 0; j < 4; j++) {
        ldsst<u32>((wbase ^ (u32)(j << 4)) + j * 256, pkh(e0[j][0], e0[j][1]));
        ldsst<u32>((wbase ^ (u32)((4 + j) << 4)) + (4 + j) * 256, pkh(e1[j][0], e1[j][1]));
      }
      f16x8 ag[4];
#pragma unroll
      for (int s = 0; s < 4; s++) ag[s] = ldsld<f16x8>(ra ^ (u32)(s << 6));
      if (shk) bsvc();
      bar();
      if (shk) bsvc();
      f32x4 d0 = {0.f, 0.f, 0.f, 0.f}, d1 = {0.f, 0.f, 0.f, 0.f};
#pragma unroll
      for (int s = 0; s < 4; s++) d0 = __builtin_amdgcn_mfma_f32_16x16x32_f16(af[s], bf[s], d0, 0, 0, 0);
#pragma unroll
      for (int s = 0; s < 4; s++) d1 = __builtin_amdgcn_mfma_f32_16x16x32_f16(ag[s], bf[s], d1, 0, 0, 0);
      const u32 PB = L_TAB + (u32)(w & 3) * 8192 - 0x3f00u * 4u;
      const float l00 = rb + rs * (float)(4 * kg) + cs * (float)rr;
#pragma unroll
      for (int e = 0; e < 4; e++) {
        float l0 = l00 + rs * (float)e, l1 = l0 + 0.5f * cs;
        float s0 = isig, s1 = isig;
        if (g > 0.f) { s0 *= __builtin_amdgcn_exp2f(g * l0); s1 *= __builtin_amdgcn_exp2f(g * l1); }
        l0 = fmaxf(l0, 0.5f);
        l0 = l0 > 2048.f ? 4096.f : l0;
        l1 = l1 < 128.f * cs ? l1 : 4096.f;
        ldsst<float>(PB + ((__float_as_uint(l0) >> 16) << 2), d0[e] * s0);
        ldsst<float>(PB + ((__float_as_uint(l1) >> 16) << 2), d1[e] * s1);
      }
      if (lane == 0 && G == 0) ldsst<float>(T + 7168, wsum * __builtin_amdgcn_exp2f(g * 2048.f));
    }
    if (shk) bsvc();
    STAMP(3);
    bar();
    if (shk) bsvc();
    float kinv;
    {
      float bnd = ldsld<float>(pb0 + 7168) + ldsld<float>(pb0 + 8192 + 7168);
      float bnd2 = ldsld<float>(L_TAB + kch * 16384 + 7168) + ldsld<float>(L_TAB + kch * 16384 + 8192 + 7168);
      float v[11];
#pragma unroll
      for (int e = 0; e < 11; e++) v[e] = ldsld<float>(ad[e]) + ldsld<float>(ad[e] + 8192);
      float ks, ki, ks2;
      pow2scale(bnd, 14, ks, ki);
      pow2scale(bnd2, 14, ks2, kinv);
      u32 PK[5], QK[5];
#pragma unroll
      for (int e = 0; e < 11; e++) v[e] *= (e >= 8 && eg == 255) ? 0.f : ks;
#pragma unroll
      for (int j = 0; j < 5; j++) { PK[j] = pkh(v[2 * j], v[2 * j + 1]); QK[j] = pkh(v[2 * j + 1], v[2 * j + 2]); }
      u32 a = L_KCP + ech * 16640 + eg * 16;
      ldsst<u32x4>(a, (u32x4){PK[0], PK[1], PK[2], PK[3]});
      ldsst<u32x4>(a + 4160, (u32x4){QK[0], QK[1], QK[2], QK[3]});
      ldsst<u32x4>(a + 8320, (u32x4){PK[1], PK[2], PK[3], PK[4]});
      ldsst<u32x4>(a + 12480, (u32x4){QK[1], QK[2], QK[3], QK[4]});
      if (tid < 16) ldsst<u32x4>(L_KCP + (tid >> 1) * 4160 + (256 + (tid & 1)) * 16, (u32x4){0, 0, 0, 0});
      else if (tid >= 64 && tid < 128) ldsst<u32x4>(L_UL + ((tid >> 5) & 1) * 4608 + (tid & 31) * 16, (u32x4){0, 0, 0, 0});
    }
    STAMP(4);
    if (shk) {
      u32 spins = 0;
      while (ust < 2 && ++spins < SPIN_MAX) { waitv<0>(); bsvc(); }
      STAMP(7);
      waitv<0>();
    }
    __builtin_amdgcn_s_setprio(2);
    bar();
    STAMP(5);
    if (w < 4) {
      const int n = rr;
      f32x4 acc[8];
#pragma unroll
      for (int a = 0; a < 8; a++) acc[a] = (f32x4){0.f, 0.f, 0.f, 0.f};
      const int cc = (3 - n) & 3;
      u32 ub = L_UL + kch * 4608 + (u32)((256 + 16 * (n - 8 * kmh) - 16 + 8 * kg) * 2);
      u32 ab = L_KCP + kch * 16640 + cc * 4160 + (u32)((2031 - n + 8 * kg - cc - 1792 - 128 * kmh) * 2);
      f16x8 Bq[8];
      u32x2 Al[4], Ah[4];
#pragma unroll
      for (int q = 0; q < 8; q++) Bq[q] = ldsld<f16x8>(ub + q * 512);
#pragma unroll
      for (int i = 0; i < 4; i++) { Al[i] = ldsldv<u32x2>(ab + (7 - i) * 512); Ah[i] = ldsldv<u32x2>(ab + (7 - i) * 512 + 8); }
#pragma unroll 1
      for (int it = 0; it < 4; it++) {
#pragma unroll
        for (int i = 0; i < 8; i++) {
          const int in = (i + 4) & 7;
          u32 a = ab + (7 - in) * 512 - (i >= 4 ? 64 : 0);
          f16x8 A = __builtin_bit_cast(f16x8, (u32x4){Al[i & 3][0], Al[i & 3][1], Ah[i & 3][0], Ah[i & 3][1]});
#pragma unroll
          for (int a2 = 7; a2 >= i; a2--) acc[a2] = __builtin_amdgcn_mfma_f32_16x16x32_f16(A, Bq[a2 - i], acc[a2], 0, 0, 0);
          SB0;
          Al[i & 3] = ldsldv<u32x2>(a); Ah[i & 3] = ldsldv<u32x2>(a + 8);
          Bq[7 - i] = ldsld<f16x8>(ub - 64 + (7 - i) * 512);
          SB0;
        }
        ub -= 64; ab -= 64;
      }
      STAMP(6);
      if (kmh) {
#pragma unroll
        for (int a = 0; a < 4; a++) { f32x4 t = acc[a]; acc[a] = acc[4 + a]; acc[4 + a] = t; asm volatile("" : "+v"(acc[a]), "+v"(acc[4 + a])); }
      }
#pragma unroll
      for (int a = 0; a < 4; a++) ldsst<f32x4>(L_RED + (u32)(w * 4096 + a * 1024 + lane * 16), acc[4 + a]);
      bar();
      const float dsk = bf2f(pk_);
      char* yp = ws + WS_Y + (size_t)dch * 4096 + kmh * 2048;
#pragma unroll
      for (int pr = 0; pr < 2; pr++) {
        u32 pk[2][2];
#pragma unroll
        for (int j = 0; j < 2; j++) {
          const int a = 2 * pr + j;
          f32x4 yy = acc[a] + ldsld<f32x4>(L_RED + (u32)((w ^ 1) * 4096 + a * 1024 + lane * 16));
          f16x4 uu = ldsld<f16x4>(L_UL + kch * 4608 + 512 + (u32)((256 * (4 * kmh + a) + 16 * n + 4 * kg) * 2));
          u32 y01 = pkbf(yy[0] * kinv, yy[1] * kinv), y23 = pkbf(yy[2] * kinv, yy[3] * kinv);
          u32 s01 = pkbf((float)uu[0] * dsk, (float)uu[1] * dsk), s23 = pkbf((float)uu[2] * dsk, (float)uu[3] * dsk);
          pk[j][0] = pkbf(bf2f(y01) + bf2f(s01), bfhi(y01) + bfhi(s01));
          pk[j][1] = pkbf(bf2f(y23) + bf2f(s23), bfhi(y23) + bfhi(s23));
        }
        auto s0 = __builtin_amdgcn_permlane16_swap(pk[0][0], pk[1][0], false, false);
        auto s1 = __builtin_amdgcn_permlane16_swap(pk[0][1], pk[1][1], false, false);
        st16c(yp, (u32)((2 * pr + (kg & 1)) * 512 + (kg >> 1) * 256 + n * 16), (u32x4){s0[0], s1[0], s0[1], s1[1]});
      }
      waitv<0>();
      if (lane == 0) st4c(ws, WS_YFLAG + (u32)(kmh * 512 + dch) * 4, EP);
      STAMP(8);
    }
  }
  STAMP_OUT();
}

extern "C" void kernel_launch(void* const* d_in, const int* in_sizes, int n_in, void* d_out, int out_size, void* d_ws,
                              size_t ws_size, hipStream_t stream) {
  fused_kernel<<<512, 512, 0, stream>>>((const u16*)d_in[0], (const u16*)d_in[1], (const u16*)d_in[2], (const u16*)d_in[3],
                                        (const u16*)d_in[4], (const u16*)d_in[5], (const u16*)d_in[6], (const u16*)d_in[7],
                                        (const u16*)d_in[8], (const u16*)d_in[10], (const u16*)d_in[11], (const u16*)d_in[12],
                                        (u16*)d_out, (char*)d_ws);
}
```

```cpp
#include <hip/hip_runtime.h>
#include <stdint.h>
#include <type_traits>

typedef unsigned short u16;
typedef unsigned int u32;
typedef unsigned long long u64;
typedef __attribute__((ext_vector_type(8))) __bf16 bf16x8;
typedef __attribute__((ext_vector_type(2))) __bf16 bf16x2;
typedef __attribute__((ext_vector_type(8))) _Float16 f16x8;
typedef __attribute__((ext_vector_type(4))) _Float16 f16x4;
typedef __attribute__((ext_vector_type(2))) _Float16 f16x2;
typedef __attribute__((ext_vector_type(2))) float f32x2;
typedef __attribute__((ext_vector_type(4))) float f32x4;
typedef __attribute__((ext_vector_type(16))) float f32x16;
typedef __attribute__((ext_vector_type(4))) u32 u32x4;
typedef __attribute__((ext_vector_type(2))) u32 u32x2;
typedef __attribute__((ext_vector_type(4))) short s16x4;
#define AS3 __attribute__((address_space(3)))
#define DEVI __device__ __forceinline__
template <int N> using IC = std::integral_constant<int, N>;

constexpr u32 WS_STAT = 0;
constexpr u32 WS_CS = 16384;
constexpr u32 WS_UFLAG = 30720;
constexpr u32 WS_YFLAG = 31744;
constexpr u32 WS_HFLAG = 35840;
constexpr u32 WS_R = 40960;
constexpr u32 WS_A = 45056;
constexpr u32 WS_N = 49152;
constexpr size_t WS_U = 1u << 20, WS_Y = 4u << 20, WS_H = 8u << 20, WS_DBG = 32u << 20;
constexpr u32 LA_XA = 16384, LA_WA = 17408, LA_RS = 18432, LA_ZERO = 18944;
constexpr u32 L_TAB = 0;
constexpr u32 L_KCP = 32768;
constexpr u32 L_UL = 66048;
constexpr u32 L_LNS = 66048;
constexpr u32 L_ST = 75264;
constexpr u32 L_RED = 0;
constexpr u32 LSPIN_MAX = 1u << 20;
constexpr u32 SPIN_MAX = 1u << 15;
constexpr u32 EP = 1u;
constexpr int LEADER = 248;
#define PMASK 0x2492

template <typename T> DEVI T ldsld(u32 a) { return *(AS3 T*)(uintptr_t)a; }
template <typename T> DEVI T ldsldv(u32 a) { return *(volatile AS3 T*)(uintptr_t)a; }
template <typename T> DEVI void ldsst(u32 a, T v) { *(AS3 T*)(uintptr_t)a = v; }
DEVI u32x2 ldstr(u32 a) { return __builtin_bit_cast(u32x2, __builtin_amdgcn_ds_read_tr16_b64_v4i16((AS3 s16x4*)(uintptr_t)a)); }
DEVI float bf2f(u32 v) { return __uint_as_float(v << 16); }
DEVI float bfhi(u32 v) { return __uint_as_float(v & 0xffff0000u); }
DEVI float rbf(float f) { return (float)(__bf16)f; }
DEVI u32 pkbf(float a, float b) { bf16x2 v = {(__bf16)a, (__bf16)b}; return __builtin_bit_cast(u32, v); }
DEVI u32 pkh(float a, float b) { f16x2 v = {(_Float16)a, (_Float16)b}; return __builtin_bit_cast(u32, v); }

template <int STEP, bool COH>
DEVI void glds2(const void* sbase, u32 v0, u32 v1, u32 lds0) {
  u32 keep;
  if (COH)
    asm volatile("s_mov_b32 %0, m0\n\ts_mov_b32 m0, %4\n\ts_nop 0\n\tglobal_load_lds_dwordx4 %1, %3 sc1\n\t"
                 "s_add_u32 m0, m0, %5\n\ts_nop 0\n\tglobal_load_lds_dwordx4 %2, %3 sc1\n\ts_mov_b32 m0, %0"
                 : "=&s"(keep) : "v"(v0), "v"(v1), "s"(sbase), "s"(lds0), "n"(STEP) : "memory", "scc");
  else
    asm volatile("s_mov_b32 %0, m0\n\ts_mov_b32 m0, %4\n\ts_nop 0\n\tglobal_load_lds_dwordx4 %1, %3\n\t"
                 "s_add_u32 m0, m0, %5\n\ts_nop 0\n\tglobal_load_lds_dwordx4 %2, %3\n\ts_mov_b32 m0, %0"
                 : "=&s"(keep) : "v"(v0), "v"(v1), "s"(sbase), "s"(lds0), "n"(STEP) : "memory", "scc");
}
template <int SSTEP, bool COH>
DEVI void glds8(const char* sbase, u32 v0, u32 v1, u32 lds0) {
#pragma unroll
  for (int j = 0; j < 4; j++) glds2<1024, COH>(sbase + (size_t)j * SSTEP, v0, v1, lds0 + j * 2048);
}
DEVI u32x4 mksrd(const void* p, u32 bytes) {
  u64 a = (u64)p;
  return (u32x4){(u32)a, (u32)(a >> 32) & 0xffffu, bytes, 0x00020000u};
}
template <bool COHA>
DEVI void dma4(u32x4 sA, u32 soA, u32 vA0, u32 vA1, u32x4 sB, u32 soB, u32 vB0, u32 vB1, u32 ldsA) {
  if (COHA)
    asm volatile("s_mov_b32 m0, %8\n\ts_nop 0\n\tbuffer_load_dwordx4 %2, %0, %1 offen sc1 lds\n\t"
                 "buffer_load_dwordx4 %3, %0, %1 offen offset:1024 sc1 lds\n\ts_add_u32 m0, m0, 0x2000\n\ts_nop 0\n\t"
                 "buffer_load_dwordx4 %6, %4, %5 offen lds\n\tbuffer_load_dwordx4 %7, %4, %5 offen offset:1024 lds"
                 ::"s"(sA), "s"(soA), "v"(vA0), "v"(vA1), "s"(sB), "s"(soB), "v"(vB0), "v"(vB1), "s"(ldsA) : "memory", "scc");
  else
    asm volatile("s_mov_b32 m0, %8\n\ts_nop 0\n\tbuffer_load_dwordx4 %2, %0, %1 offen lds\n\t"
                 "buffer_load_dwordx4 %3, %0, %1 offen offset:1024 lds\n\ts_add_u32 m0, m0, 0x2000\n\ts_nop 0\n\t"
                 "buffer_load_dwordx4 %6, %4, %5 offen lds\n\tbuffer_load_dwordx4 %7, %4, %5 offen offset:1024 lds"
                 ::"s"(sA), "s"(soA), "v"(vA0), "v"(vA1), "s"(sB), "s"(soB), "v"(vB0), "v"(vB1), "s"(ldsA) : "memory", "scc");
}
template <bool COH>
DEVI void dma2(u32x4 sA, u32 soA, u32 vA0, u32 vA1, u32 ldsA) {
  if (COH)
    asm volatile("s_mov_b32 m0, %4\n\ts_nop 0\n\tbuffer_load_dwordx4 %2, %0, %1 offen sc1 lds\n\t"
                 "buffer_load_dwordx4 %3, %0, %1 offen offset:1024 sc1 lds" ::"s"(sA), "s"(soA), "v"(vA0), "v"(vA1), "s"(ldsA) : "memory");
  else
    asm volatile("s_mov_b32 m0, %4\n\ts_nop 0\n\tbuffer_load_dwordx4 %2, %0, %1 offen lds\n\t"
                 "buffer_load_dwordx4 %3, %0, %1 offen offset:1024 lds" ::"s"(sA), "s"(soA), "v"(vA0), "v"(vA1), "s"(ldsA) : "memory");
}
template <bool COHA>
DEVI void dma11(u32x4 sA, u32 soA, u32 vA, u32x4 sB, u32 soB, u32 vB, u32 ldsA) {
  if (COHA)
    asm volatile("s_mov_b32 m0, %6\n\ts_nop 0\n\tbuffer_load_dwordx4 %2, %0, %1 offen sc1 lds\n\ts_add_u32 m0, m0, 0x2000\n\ts_nop 0\n\t"
                 "buffer_load_dwordx4 %5, %3, %4 offen lds" ::"s"(sA), "s"(soA), "v"(vA), "s"(sB), "s"(soB), "v"(vB), "s"(ldsA) : "memory", "scc");
  else
    asm volatile("s_mov_b32 m0, %6\n\ts_nop 0\n\tbuffer_load_dwordx4 %2, %0, %1 offen lds\n\ts_add_u32 m0, m0, 0x2000\n\ts_nop 0\n\t"
                 "buffer_load_dwordx4 %5, %3, %4 offen lds" ::"s"(sA), "s"(soA), "v"(vA), "s"(sB), "s"(soB), "v"(vB), "s"(ldsA) : "memory", "scc");
}
template <bool COH>
DEVI void dma1(u32x4 sA, u32 soA, u32 vA, u32 ldsA) {
  if (COH) asm volatile("s_mov_b32 m0, %3\n\ts_nop 0\n\tbuffer_load_dwordx4 %2, %0, %1 offen sc1 lds" ::"s"(sA), "s"(soA), "v"(vA), "s"(ldsA) : "memory");
  else asm volatile("s_mov_b32 m0, %3\n\ts_nop 0\n\tbuffer_load_dwordx4 %2, %0, %1 offen lds" ::"s"(sA), "s"(soA), "v"(vA), "s"(ldsA) : "memory");
}
template <bool COHA>
DEVI void dma21(u32x4 sA, u32 soA, u32 vA0, u32 vA1, u32x4 sB, u32 soB, u32 vB, u32 ldsA, u32 ldsB) {
  if (COHA)
    asm volatile("s_mov_b32 m0, %7\n\ts_nop 0\n\tbuffer_load_dwordx4 %2, %0, %1 offen sc1 lds\n\t"
                 "buffer_load_dwordx4 %3, %0, %1 offen offset:1024 sc1 lds\n\ts_mov_b32 m0, %8\n\ts_nop 0\n\tbuffer_load_dwordx4 %6, %4, %5 offen lds"
                 ::"s"(sA), "s"(soA), "v"(vA0), "v"(vA1), "s"(sB), "s"(soB), "v"(vB), "s"(ldsA), "s"(ldsB) : "memory");
  else
    asm volatile("s_mov_b32 m0, %7\n\ts_nop 0\n\tbuffer_load_dwordx4 %2, %0, %1 offen lds\n\t"
                 "buffer_load_dwordx4 %3, %0, %1 offen offset:1024 lds\n\ts_mov_b32 m0, %8\n\ts_nop 0\n\tbuffer_load_dwordx4 %6, %4, %5 offen lds"
                 ::"s"(sA), "s"(soA), "v"(vA0), "v"(vA1), "s"(sB), "s"(soB), "v"(vB), "s"(ldsA), "s"(ldsB) : "memory");
}
DEVI void rawbar() { asm volatile("" ::: "memory"); __builtin_amdgcn_s_barrier(); asm volatile("" ::: "memory"); }
DEVI void st16c(void* sbase, u32 voff, u32x4 v) {
  asm volatile("global_store_dwordx4 %0, %1, %2 sc1\n\ts_nop 1" ::"v"(voff), "v"(v), "s"(sbase) : "memory");
}
DEVI void st16(void* sbase, u32 voff, u32x4 v) {
  asm volatile("global_store_dwordx4 %0, %1, %2\n\ts_nop 1" ::"v"(voff), "v"(v), "s"(sbase) : "memory");
}
DEVI void st8c(void* sbase, u32 voff, u32x2 v) {
  asm volatile("global_store_dwordx2 %0, %1, %2 sc1\n\ts_nop 1" ::"v"(voff), "v"(v), "s"(sbase) : "memory");
}
DEVI void st4c(void* sbase, u32 voff, u32 v) {
  asm volatile("global_store_dword %0, %1, %2 sc1\n\ts_nop 1" ::"v"(voff), "v"(v), "s"(sbase) : "memory");
}
DEVI u32 ld4c_wait(const void* sbase, u32 voff) {
  u32 r;
  asm volatile("global_load_dword %0, %1, %2 sc1\n\ts_waitcnt vmcnt(0)" : "=&v"(r) : "v"(voff), "s"(sbase) : "memory");
  return r;
}
DEVI u32x4 ld16c_wait(const void* sbase, u32 voff) {
  u32x4 r;
  asm volatile("global_load_dwordx4 %0, %1, %2 sc1\n\ts_waitcnt vmcnt(0)" : "=&v"(r) : "v"(voff), "s"(sbase) : "memory");
  return r;
}
template <int N> DEVI void waitv() { asm volatile("s_waitcnt vmcnt(%0)" ::"n"(N) : "memory"); }
DEVI void bar() {
  asm volatile("s_waitcnt lgkmcnt(0)" ::: "memory");
  __builtin_amdgcn_s_barrier();
  asm volatile("" ::: "memory");
}
template <int CTRL, int RM> DEVI float dppf(float old, float v) {
  return __int_as_float(__builtin_amdgcn_update_dpp(__float_as_int(old), __float_as_int(v), CTRL, RM, 0xf, false));
}
DEVI float wave_sum(float v) {
  v += dppf<0x111, 0xf>(0.f, v); v += dppf<0x112, 0xf>(0.f, v); v += dppf<0x114, 0xf>(0.f, v); v += dppf<0x118, 0xf>(0.f, v);
  v += dppf<0x142, 0xa>(0.f, v); v += dppf<0x143, 0xc>(0.f, v);
  return __int_as_float(__builtin_amdgcn_readlane(__float_as_int(v), 63));
}
DEVI float wave_max0(float v) {
  v = fmaxf(v, dppf<0x111, 0xf>(0.f, v)); v = fmaxf(v, dppf<0x112, 0xf>(0.f, v)); v = fmaxf(v, dppf<0x114, 0xf>(0.f, v));
  v = fmaxf(v, dppf<0x118, 0xf>(0.f, v)); v = fmaxf(v, dppf<0x142, 0xa>(0.f, v)); v = fmaxf(v, dppf<0x143, 0xc>(0.f, v));
  return __int_as_float(__builtin_amdgcn_readlane(__float_as_int(v), 63));
}
#define CM_MUL(t, a, b) "v_pk_mul_f32 " t ", " a ", " b " op_sel_hi:[0,1]\n\t"
#define CM_FMA(r, a, b, t) "v_pk_fma_f32 " r ", " a ", " b ", " t " op_sel:[1,1,0] op_sel_hi:[1,0,1] neg_lo:[1,0,0]\n\t"
DEVI f32x2 cmul(f32x2 a, f32x2 b) {
  f32x2 t, r;
  asm(CM_MUL("%0", "%1", "%2") : "=v"(t) : "v"(a), "v"(b));
  asm(CM_FMA("%0", "%1", "%2", "%3") : "=v"(r) : "v"(a), "v"(b), "v"(t));
  return r;
}
DEVI void cmul4(f32x2& a0, f32x2& a1, f32x2& a2, f32x2& a3, f32x2 z) {
  f32x2 t0, t1, t2, t3;
  asm(CM_MUL("%0", "%4", "%8") CM_MUL("%1", "%5", "%8") CM_MUL("%2", "%6", "%8") CM_MUL("%3", "%7", "%8")
      CM_FMA("%0", "%4", "%8", "%0") CM_FMA("%1", "%5", "%8", "%1") CM_FMA("%2", "%6", "%8", "%2") CM_FMA("%3", "%7", "%8", "%3")
      : "=&v"(t0), "=&v"(t1), "=&v"(t2), "=&v"(t3) : "v"(a0), "v"(a1), "v"(a2), "v"(a3), "v"(z));
  a0 = t0; a1 = t1; a2 = t2; a3 = t3;
}
DEVI f32x2 cpow(float a2, float fhi, float flo, float R) {
  float m = __builtin_amdgcn_exp2f(a2 * R);
  float ph = __builtin_fmaf(flo, R, __builtin_amdgcn_fractf(fhi * R));
  return (f32x2){m * __builtin_amdgcn_cosf(ph), m * __builtin_amdgcn_sinf(ph)};
}
DEVI void pow2scale(float mx, int target, float& s, float& inv) {
  int ex = (int)((__float_as_uint(mx) >> 23) & 0xff);
  int se = 254 + target - ex;
  se = se < 2 ? 2 : (se > 252 ? 252 : se);
  s = __uint_as_float((u32)se << 23);
  inv = __uint_as_float((u32)(254 - se) << 23);
}

DEVI void sig(u32 a, u32 v) {
  asm volatile("s_mov_b64 exec, 1\n\tds_write_b32 %0, %1\n\ts_mov_b64 exec, -1" ::"v"(a), "v"(v) : "memory");
}
DEVI u32 min4(u32x4 f) { return __builtin_amdgcn_readfirstlane(min(min(f[0], f[1]), min(f[2], f[3]))); }
DEVI void wait4(u32 a, u32 v) {
  u32 spins = 0;
  while (min4(ldsldv<u32x4>(a)) < v && ++spins < LSPIN_MAX) __builtin_amdgcn_s_sleep(1);
  asm volatile("" ::: "memory");
}
DEVI u32 min2(u32x2 f) { return __builtin_amdgcn_readfirstlane(min(f[0], f[1])); }
DEVI void wait2(u32 a, u32 v) {
  u32 spins = 0;
  while (min2(ldsldv<u32x2>(a)) < v && ++spins < LSPIN_MAX) {}
  asm volatile("" ::: "memory");
}
DEVI void wait4f(u32 a, u32 v) {
  u32 spins = 0;
  while (min4(ldsldv<u32x4>(a)) < v && ++spins < LSPIN_MAX) {}
  asm volatile("" ::: "memory");
}
DEVI void wait8(u32 a, u32 v) {
  u32 spins = 0;
  for (;;) {
    u32x4 f0 = ldsldv<u32x4>(a), f1 = ldsldv<u32x4>(a + 16);
    if (min(min4(f0), min4(f1)) >= v || ++spins > LSPIN_MAX) break;
  }
  asm volatile("" ::: "memory");
}
DEVI void wait1(u32 a, u32 v) {
  u32 spins = 0;
  while (__builtin_amdgcn_readfirstlane(ldsldv<u32>(a)) < v && ++spins < LSPIN_MAX) __builtin_amdgcn_s_sleep(1);
  asm volatile("" ::: "memory");
}

__global__ __launch_bounds__(512, 4) void fused_kernel(
    const u16* __restrict__ x, const u16* __restrict__ Are, const u16* __restrict__ Aim, const u16* __restrict__ Bre,
    const u16* __restrict__ Bim, const u16* __restrict__ Cre, const u16* __restrict__ Cim, const u16* __restrict__ ldt,
    const u16* __restrict__ Dsk, const u16* __restrict__ w1, const u16* __restrict__ b1, const u16* __restrict__ w2,
    u16* __restrict__ out, char* __restrict__ ws) {
  __shared__ __attribute__((aligned(16))) char sm[81920];
  asm volatile("" ::"v"(sm) : "memory");
  const int tid = threadIdx.x, lane = tid & 63;
  const int w = __builtin_amdgcn_readfirstlane(tid >> 6);
  const int gb = blockIdx.x, b = gb & 255, xcd = b & 7, bi = b >> 3;
  const bool isg1 = bi < 28;
  const int bm = 2 * xcd + (bi & 1), bn = bi >> 1;
  const int tb = 32 * xcd + bi;
#define STAMP(i)
#define STAMP_OUT()
#define SB0 __builtin_amdgcn_sched_barrier(0)
  STAMP(0);
  const u32 ao = lane == 0 ? WS_N + (u32)(gb & 31) * 128u : WS_A + (u32)gb * 4;
#define SHK_POLL() asm volatile("global_load_dword v127, %0, %1 sc1" ::"v"(ao), "s"(ws) : "memory", "v127")
  u32 rb_ = 0, nsent = 0;
  bool rel = false;
  auto shk_first = [&]() {
    u32 n, a;
    asm volatile("v_readlane_b32 %0, v127, 0\n\tv_readlane_b32 %1, v127, 1" : "=s"(n), "=s"(a)::"memory");
    rb_ = a + 1u; nsent = n;
    if (lane == 0) st8c(ws, WS_R + (u32)gb * 8, (u32x2){rb_, n});
    SHK_POLL();
  };
  auto shk_step = [&]() {
    u32 n, a;
    asm volatile("v_readlane_b32 %0, v127, 0\n\tv_readlane_b32 %1, v127, 1" : "=s"(n), "=s"(a)::"memory");
    if (a == rb_) rel = true;
    else if (n != nsent) { nsent = n; if (lane == 0) st8c(ws, WS_R + (u32)gb * 8, (u32x2){rb_, n}); }
    SHK_POLL();
  };
  auto shk_finish = [&]() {
    u32 spins = 0;
    while (!rel && ++spins < SPIN_MAX) { waitv<0>(); shk_step(); }
    waitv<0>();
  };
  if (gb < 256) {
    const int bm2 = 4 * xcd + (bi & 3), bn2 = bi >> 2;
    const int ci = (7 - xcd) * 4 + (bi - 28);
    if (w < 4) {
      __builtin_amdgcn_s_setprio(3);
      const int hh = lane >> 5, blk = (lane >> 4) & 1, q4 = (lane & 15) >> 2, p4 = lane & 3;
      u32 sb = 0;
      if (isg1) {
        const int wf = w & 1, wt = w >> 1;
        const int tl = wt * 64 + (lane & 31);
        const u32 xa0 = (u32)(tl * 64 + ((hh ^ ((tl >> 2) & 3)) << 4));
        const u32 wa0 = (u32)(8192 + (8 * hh + q4) * 256 +
                              (((8 * wf + 4 * (p4 & 1) + blk) ^ (((q4 & 1) << 1) | ((q4 & 2) << 2))) << 4) + 8 * (p4 >> 1));
        f32x16 acc[2][2];
#pragma unroll
        for (int i = 0; i < 16; i++) { acc[0][0][i] = 0.f; acc[0][1][i] = 0.f; acc[1][0][i] = 0.f; acc[1][1][i] = 0.f; }
        bf16x8 FX[3][2], FW[3][2];
        auto half = [&](auto C_, auto R_, auto KS_) {
          constexpr int C = decltype(C_)::value, R = decltype(R_)::value, ks = decltype(KS_)::value;
          const u32 ax = sb + (xa0 ^ (u32)(ks << 5)), aw0 = sb + wa0 + ks * 4096, aw1 = sb + (wa0 ^ 32u) + ks * 4096;
          u32x2 a0, a1, a2, a3;
          SB0; acc[0][0] = __builtin_amdgcn_mfma_f32_32x32x16_bf16(FW[C][0], FX[C][0], acc[0][0], 0, 0, 0); SB0;
          FX[R][0] = ldsld<bf16x8>(ax); a0 = ldstr(aw0);
          SB0; acc[0][1] = __builtin_amdgcn_mfma_f32_32x32x16_bf16(FW[C][0], FX[C][1], acc[0][1], 0, 0, 0); SB0;
          a1 = ldstr(aw0 + 1024); FX[R][1] = ldsld<bf16x8>(ax + 2048);
          SB0; acc[1][0] = __builtin_amdgcn_mfma_f32_32x32x16_bf16(FW[C][1], FX[C][0], acc[1][0], 0, 0, 0); SB0;
          a2 = ldstr(aw1); a3 = ldstr(aw1 + 1024);
          SB0; acc[1][1] = __builtin_amdgcn_mfma_f32_32x32x16_bf16(FW[C][1], FX[C][1], acc[1][1], 0, 0, 0); SB0;
          FW[R][0] = __builtin_bit_cast(bf16x8, (u32x4){a0[0], a0[1], a1[0], a1[1]});
          FW[R][1] = __builtin_bit_cast(bf16x8, (u32x4){a2[0], a2[1], a3[0], a3[1]});
        };
        auto step = [&](auto A_, auto B_, auto C_) {
          rawbar();
          sb = sb == 65536u ? 0u : sb + 16384u;
          half(A_, C_, IC<0>{}); half(B_, A_, IC<1>{});
        };
        rawbar();
        STAMP(1);
#pragma unroll
        for (int ks = 0; ks < 2; ks++)
#pragma unroll
          for (int j = 0; j < 2; j++) {
            FX[ks][j] = ldsld<bf16x8>((xa0 ^ (u32)(ks << 5)) + j * 2048);
            u32 a = (wa0 ^ (u32)(j << 5)) + ks * 4096;
            u32x2 a0 = ldstr(a), a1 = ldstr(a + 1024);
            FW[ks][j] = __builtin_bit_cast(bf16x8, (u32x4){a0[0], a0[1], a1[0], a1[1]});
          }
#pragma unroll 1
        for (int s = 0; s < 15; s += 3) { step(IC<0>{}, IC<1>{}, IC<2>{}); step(IC<2>{}, IC<0>{}, IC<1>{}); step(IC<1>{}, IC<2>{}, IC<0>{}); }
#pragma unroll
        for (int ks = 0; ks < 2; ks++)
#pragma unroll
          for (int tau = 0; tau < 2; tau++)
#pragma unroll
            for (int j = 0; j < 2; j++)
              acc[tau][j] = __builtin_amdgcn_mfma_f32_32x32x16_bf16(FW[ks][tau], FX[ks][j], acc[tau][j], 0, 0, 0);
        STAMP(2);
        bar();
        STAMP(3);
        int l2 = lane;
        asm volatile("" : "+v"(l2));
        const int hh2 = l2 >> 5, tl2 = wt * 64 + (l2 & 31);
        {
          const int li = l2 & 15;
          const u32 fo = (u32)(wf * 64 + 32 * ((li >> 2) & 1) + 8 * ((l2 >> 4) & 1) + 4 * (li >> 3) + (li & 3));
          bf16x8 xa[2], wa[2];
#pragma unroll
          for (int j = 0; j < 2; j++) { u32x2 t = ldsld<u32x2>(hh2 ? LA_ZERO : LA_XA + (tl2 + 32 * j) * 8); xa[j] = __builtin_bit_cast(bf16x8, (u32x4){t[0], t[1], 0u, 0u}); }
#pragma unroll
          for (int tau = 0; tau < 2; tau++) { u32x2 t = ldsld<u32x2>(hh2 ? LA_ZERO : LA_WA + (fo + 16 * tau) * 8); wa[tau] = __builtin_bit_cast(bf16x8, (u32x4){t[0], t[1], 0u, 0u}); }
#pragma unroll
          for (int tau = 0; tau < 2; tau++)
#pragma unroll
            for (int j = 0; j < 2; j++) acc[tau][j] = __builtin_amdgcn_mfma_f32_32x32x16_bf16(wa[tau], xa[j], acc[tau][j], 0, 0, 0);
        }
        char* hp = ws + WS_H + (size_t)(bm * 2 + wt) * 229376 + (bn * 16 + wf * 8) * 1024;
#pragma unroll
        for (int j = 0; j < 2; j++) {
          const float r = ldsld<float>(LA_RS + (tl2 + 32 * j) * 4);
          const float c1 = r * 1.44269504088896341f, m2r = -2.f * r;
          const f32x2 c1v = {c1, c1}, rv = {r, r}, m2v = {m2r, m2r}, one = {1.f, 1.f};
#pragma unroll
          for (int tau = 0; tau < 2; tau++) {
#pragma unroll
            for (int hf = 0; hf < 2; hf++) {
              f32x2 av[4], t[4];
#pragma unroll
              for (int jj = 0; jj < 4; jj++) { av[jj] = (f32x2){acc[tau][j][8 * hf + 2 * jj], acc[tau][j][8 * hf + 2 * jj + 1]}; t[jj] = av[jj] * c1v; }
#pragma unroll
              for (int jj = 0; jj < 4; jj++) t[jj] = (f32x2){__builtin_amdgcn_exp2f(t[jj][0]), __builtin_amdgcn_exp2f(t[jj][1])};
#pragma unroll
              for (int jj = 0; jj < 4; jj++) t[jj] = t[jj] + one;
#pragma unroll
              for (int jj = 0; jj < 4; jj++) t[jj] = __builtin_elementwise_fma(t[jj], t[jj], one);
#pragma unroll
              for (int jj = 0; jj < 4; jj++) t[jj] = (f32x2){__builtin_amdgcn_rcpf(t[jj][0]), __builtin_amdgcn_rcpf(t[jj][1])};
#pragma unroll
              for (int jj = 0; jj < 4; jj++) t[jj] = __builtin_elementwise_fma(t[jj], m2v, rv);
              u32x4 o;
#pragma unroll
              for (int jj = 0; jj < 4; jj++) { f32x2 ov = av[jj] * t[jj]; o[jj] = pkbf(ov[0], ov[1]); }
              st16c(hp, (u32)((4 * hh2 + 2 * tau + hf) * 1024 + ((l2 & 31) + 32 * j) * 16), o);
            }
          }
        }
        STAMP(4);
        waitv<0>();
        if (l2 == 0) st4c(ws, WS_HFLAG + (u32)((bm * 2 + wt) * 32 + bn * 2 + wf) * 4, EP);
        STAMP(5);
        bar();
      } else {
        const bool lead = b == LEADER && w == 1;
        u32 cand = 0;
        u32x4 r0, r1, r2, r3;
#define LEAD_LOAD() asm volatile("global_load_dwordx4 %0, %4, %5 sc1\n\tglobal_load_dwordx4 %1, %4, %5 offset:16 sc1\n\t" \
                       "global_load_dwordx4 %2, %4, %5 offset:32 sc1\n\tglobal_load_dwordx4 %3, %4, %5 offset:48 sc1\n\ts_waitcnt vmcnt(0)" \
                       : "=&v"(r0), "=&v"(r1), "=&v"(r2), "=&v"(r3) : "v"((u32)lane * 64), "s"(ws + WS_R) : "memory")
        if (lead) {
          u32 nold;
          asm volatile("global_load_dword %0, %1, %2 sc1" : "=&v"(nold) : "v"(WS_N + (u32)(lane & 31) * 128u), "s"(ws) : "memory");
          LEAD_LOAD();
          asm volatile("" : "+v"(nold)::"memory");
          cand = __builtin_amdgcn_readfirstlane(nold) + 1u;
          while (__any(nold == cand || r0[1] == cand || r0[3] == cand || r1[1] == cand || r1[3] == cand || r2[1] == cand ||
                       r2[3] == cand || r3[1] == cand || r3[3] == cand)) cand++;
          if (lane < 32) st4c(ws, WS_N + (u32)lane * 128u, cand);
          STAMP(10);
        }
        rawbar();
        if (ci < 28) {
          float cs[8];
#pragma unroll
          for (int e = 0; e < 8; e++) cs[e] = 0.f;
#pragma unroll 4
          for (int j = 0; j < 16; j++) {
            u32x4 cw = ldsld<u32x4>((u32)(j * 4096 + tid * 16));
#pragma unroll
            for (int e = 0; e < 4; e++) { cs[2 * e] += bf2f(cw[e]); cs[2 * e + 1] += bfhi(cw[e]); }
          }
          ldsst<f32x4>((u32)(65536 + tid * 32), (f32x4){cs[0], cs[1], cs[2], cs[3]});
          ldsst<f32x4>((u32)(65536 + tid * 32 + 16), (f32x4){cs[4], cs[5], cs[6], cs[7]});
        }
        bar();
        if (ci < 28 && w == 0) {
          float s = 0.f;
#pragma unroll 8
          for (int r = 0; r < 32; r++) s += ldsld<float>((u32)(65536 + r * 256 + lane * 4));
          st8c(ws, WS_CS + (u32)(ci * 512 + lane * 8), (u32x2){__float_as_uint(s), EP});
        }
        if (lead) {
          u32 spins = 0;
          for (;;) {
            LEAD_LOAD();
            bool ok = r0[1] == cand && r0[3] == cand && r1[1] == cand && r1[3] == cand && r2[1] == cand && r2[3] == cand &&
                      r3[1] == cand && r3[3] == cand;
            if (__all(ok) || ++spins > SPIN_MAX) break;
          }
          st16c(ws + WS_A, (u32)lane * 32, (u32x4){r0[0], r0[2], r1[0], r1[2]});
          st16c(ws + WS_A, (u32)lane * 32 + 16, (u32x4){r2[0], r2[2], r3[0], r3[2]});
          STAMP(11);
        }
      }
      int l3 = lane;
      asm volatile("" : "+v"(l3));
      const int h3 = l3 >> 5, blk3 = (l3 >> 4) & 1, q3 = (l3 & 15) >> 2, p3 = l3 & 3;
      sb = 16384u;
      const int wd = w & 1, wt2 = w >> 1;
      const int tl = wt2 * 32 + (l3 & 31);
      const u32 ha0 = (u32)(h3 * 1024 + tl * 16);
      const u32 wa0 = (u32)(8192 + (8 * h3 + q3) * 128 + (((4 * wd + 2 * (p3 & 1) + blk3) ^ ((q3 & 2) << 1)) << 4) + 8 * (p3 >> 1));
      const int dl = wd * 32 + 16 * h3;
      f32x16 acc;
#pragma unroll
      for (int i = 0; i < 16; i++) acc[i] = 0.f;
      bf16x8 FH[2][4], FW[2][4];
      auto rdf2 = [&](auto P_, int ks) {
        constexpr int P = decltype(P_)::value;
        FH[P][ks] = ldsld<bf16x8>(sb + ha0 + ks * 2048);
        u32 a = sb + wa0 + ks * 2048;
        u32x2 a0 = ldstr(a), a1 = ldstr(a + 512);
        FW[P][ks] = __builtin_bit_cast(bf16x8, (u32x4){a0[0], a0[1], a1[0], a1[1]});
      };
      auto step2 = [&](auto P_) {
        constexpr int P = decltype(P_)::value;
        rawbar();
        sb = sb == 65536u ? 0u : sb + 16384u;
#pragma unroll
        for (int ks = 0; ks < 4; ks++) {
          SB0; acc = __builtin_amdgcn_mfma_f32_32x32x16_bf16(FW[P][ks], FH[P][ks], acc, 0, 0, 0); SB0;
          rdf2(IC<1 - P>{}, ks);
        }
      };
      bar();
      STAMP(6);
      rawbar();
      STAMP(7);
#pragma unroll
      for (int ks = 0; ks < 4; ks++) rdf2(IC<0>{}, ks);
#pragma unroll 1
      for (int s = 0; s < 26; s += 2) { step2(IC<0>{}); step2(IC<1>{}); }
      step2(IC<0>{});
#pragma unroll
      for (int ks = 0; ks < 4; ks++) acc = __builtin_amdgcn_mfma_f32_32x32x16_bf16(FW[1][ks], FH[1][ks], acc, 0, 0, 0);
      STAMP(8);
      rawbar();
      const u32 LA_YT = 65536u, LA_OUT = 73728u;
      u32 ya = LA_YT + (u32)((dl + q3) * 128 + (p3 >> 1) * 64 + (wt2 * 2 + blk3) * 16 + (p3 & 1) * 8);
#pragma unroll
      for (int hf = 0; hf < 2; hf++) {
        u32x2 y0 = ldstr(ya + hf * 1024), y1 = ldstr(ya + hf * 1024 + 512);
        u32x4 yy = {y0[0], y0[1], y1[0], y1[1]};
        const u32 oa = LA_OUT + (u32)(tl * 128 + (((4 * wd + 2 * h3 + hf) ^ (tl & 7)) << 4));
        u32x4 xv = ldsld<u32x4>(oa);
        u32x4 ov;
#pragma unroll
        for (int j = 0; j < 4; j++) {
          u32 s1 = pkbf(bf2f(xv[j]) + bf2f(yy[j]), bfhi(xv[j]) + bfhi(yy[j]));
          u32 s2 = pkbf(acc[8 * hf + 2 * j], acc[8 * hf + 2 * j + 1]);
          ov[j] = pkbf(bf2f(s1) + bf2f(s2), bfhi(s1) + bfhi(s2));
        }
        ldsst<u32x4>(oa, ov);
      }
#pragma unroll
      for (int j = 0; j < 2; j++) {
        int t = wt2 * 32 + (l3 >> 2) + 16 * j, c = 4 * wd + (l3 & 3);
        u32x4 ov = ldsld<u32x4>(LA_OUT + (u32)(t * 128 + ((c ^ (t & 7)) << 4)));
        st16((char*)out + (size_t)bm2 * 65536 + bn2 * 128, (u32)(t * 1024 + c * 16), ov);
      }
      STAMP(9);
    } else {
      __builtin_amdgcn_s_setprio(2);
      const int lw = w - 4;
      const bool shk = lw == 3;
      const u32 rot2 = (u32)((bi * 9) % 28), rot1 = (u32)(2 * ((2 * bm + bn) & 7));
      u32 v2, vX, vW;
      {
        const int row2 = 16 * lw + (lane >> 3);
        v2 = (u32)(row2 * 1024 + (((lane & 7) ^ ((row2 & 2) << 1)) << 4) + bn2 * 128);
        int row = 32 * lw + (lane >> 2);
        vX = (u32)((bm * 128 + row) * 1024 + (((lane & 3) ^ ((row >> 2) & 3)) << 4));
        int k = 8 * lw + (lane >> 4);
        vW = (u32)(k * 3584 + (((lane & 15) ^ (((k & 1) << 1) | ((k & 2) << 2))) << 4) + bn * 256);
      }
      const u32x4 sW2 = mksrd(w2, 1792u * 1024u), sX = mksrd(x, 2048u * 1024u), sW1 = mksrd(w1, 512u * 3584u);
      u32 so2 = rot2 * 65536u, soX = rot1 * 64u, soW = rot1 * 114688u, sl = (u32)(lw * 2048);
      const u32 slend = 81920u + (u32)(lw * 2048);
      auto nextsl = [&]() { sl += 16384u; sl = sl == slend ? sl - 81920u : sl; };
      auto next2 = [&]() { so2 += 65536u; so2 = so2 == 28u * 65536u ? 0u : so2; };
      auto i1 = [&]() {
        dma4<false>(sX, soX, vX, vX + 15360u, sW1, soW, vW, vW + 13312u, sl);
        soX = (soX + 64u) & 1023u;
        soW += 114688u; soW = soW == 16u * 114688u ? 0u : soW;
        nextsl();
      };
      auto iw2 = [&]() { dma2<false>(sW2, so2, v2, v2 + 7168u, sl + 8192u); next2(); nextsl(); };
      const char* sb_ = ws + WS_STAT + bm * 1024;
      const char* cb_ = ws + WS_CS + bn * 1024;
#define HLP_POLL() asm volatile("global_load_dwordx4 v[116:119], %0, %1 sc1\n\tglobal_load_dwordx4 v[120:123], %0, %2 sc1" \
                                ::"v"((u32)lane * 16), "s"(sb_), "s"(cb_) : "memory", "v116", "v117", "v118", "v119", "v120", "v121", "v122", "v123")
      if (isg1) {
        if (shk) {
          SHK_POLL();
          asm volatile("global_load_dword v124, %0, %1" ::"v"((u32)lane * 4), "s"(b1 + bn * 128) : "memory", "v124");
        }
#pragma unroll 1
        for (int q = 0; q < 5; q++) i1();
        waitv<16>(); rawbar();
        if (shk) {
          bool asked = false;
          shk_first();
          waitv<12>(); rawbar();
#pragma unroll 1
          for (int s = 1; s <= 14; s++) {
            if (s >= 13) waitv<4>(); else waitv<8>();
            rawbar();
            if (s <= 11) i1(); else iw2();
            if (!rel) { if (PMASK >> s & 1) shk_step(); }
            else if (!asked) { HLP_POLL(); asked = true; }
          }
          STAMP(1);
          shk_finish();
          STAMP(2);
          u32 spins = 0;
          for (;;) {
            if (!asked) HLP_POLL();
            asked = false;
            waitv<0>();
            u64 m0_, m1_, m2_, m3_;
            asm volatile("v_cmp_ne_u32 %0, 1, v117\n\tv_cmp_ne_u32 %1, 1, v119\n\tv_cmp_ne_u32 %2, 1, v121\n\tv_cmp_ne_u32 %3, 1, v123"
                         : "=&s"(m0_), "=&s"(m1_), "=&s"(m2_), "=&s"(m3_)::"memory");
            if ((m0_ | m1_ | m2_ | m3_) == 0 || ++spins > SPIN_MAX) break;
          }
          u32 g0, g1, c0, c1, bv;
          asm volatile("v_mov_b32 %0, v116\n\tv_mov_b32 %1, v118\n\tv_mov_b32 %2, v120\n\tv_mov_b32 %3, v122\n\tv_mov_b32 %4, v124"
                       : "=&v"(g0), "=&v"(g1), "=&v"(c0), "=&v"(c1), "=&v"(bv)::"memory");
          u32 n0 = (g0 & 0xffffu) ^ 0x8000u, n1 = (g1 & 0xffffu) ^ 0x8000u;
          ldsst<u32x4>(LA_XA + lane * 16, (u32x4){n0 | (n0 << 16), g0 >> 16, n1 | (n1 << 16), g1 >> 16});
          ldsst<f32x2>(LA_RS + lane * 8, (f32x2){1.f / bfhi(g0), 1.f / bfhi(g1)});
          float f0 = __uint_as_float(c0), f1 = __uint_as_float(c1), h0 = rbf(f0), h1 = rbf(f1);
          ldsst<u32x4>(LA_WA + lane * 16, (u32x4){pkbf(h0, f0 - h0), bv & 0xffffu, pkbf(h1, f1 - h1), bv >> 16});
          if (lane < 2) ldsst<u32>(LA_ZERO + lane * 4, 0u);
          STAMP(3);
        } else {
          waitv<12>(); rawbar();
#pragma unroll 1
          for (int s = 1; s <= 11; s++) { waitv<8>(); rawbar(); i1(); }
          waitv<8>(); rawbar(); iw2(); waitv<6>(); rawbar(); iw2(); waitv<4>(); rawbar(); iw2();
        }
        bar();
        iw2(); iw2();
        rawbar();
      } else {
        if (shk) {
          if (ci < 28) st4c(ws, WS_CS + (u32)(ci * 512 + lane * 8 + 4), 0u);
          SHK_POLL();
        }
        if (ci < 28) {
          const u32 vc = (u32)((16 * lw + (lane >> 3)) * 3584 + (lane & 7) * 16 + ci * 128);
#pragma unroll 1
          for (int j = 0; j < 8; j++) dma2<false>(sW1, (u32)j * 64u * 3584u, vc, vc + 8u * 3584u - 1024u, (u32)(j * 8192 + lw * 2048));
        }
        waitv<0>();
        rawbar();
        if (shk) shk_first();
        rawbar();
        sl += 16384u;
#pragma unroll 1
        for (int q = 0; q < 5; q++) iw2();
        if (shk) shk_finish();
      }
      if (shk) {
        u32 spins = 0;
        for (;;) {
          u32 f = ld4c_wait(ws + WS_HFLAG + bm2 * 128, (u32)(lane < 28 ? lane : 0) * 4);
          if (__all(f == EP) || ++spins > SPIN_MAX) break;
          __builtin_amdgcn_s_sleep(8);
        }
        STAMP(4);
      }
      const u32x4 sH = mksrd(ws + WS_H + (size_t)bm2 * 229376, 229376u);
      const u32 vH = (u32)(lw * 2048 + lane * 16);
      u32 soH = rot2 * 8192u;
      auto nextH = [&]() { soH += 8192u; soH = soH == 229376u ? 0u : soH; };
      auto i2 = [&]() { dma4<true>(sH, soH, vH, vH, sW2, so2, v2, v2 + 7168u, sl); nextH(); next2(); nextsl(); };
      rawbar();
#pragma unroll 1
      for (int q = 0; q < 5; q++) { dma2<true>(sH, soH, vH, vH, sl); nextH(); nextsl(); }
      waitv<8>(); rawbar();
      waitv<6>(); rawbar();
      waitv<4>(); rawbar(); i2();
      waitv<6>(); rawbar(); i2();
      const int ya_ = bm2 >> 2;
      const char* yfb = ws + WS_YFLAG + ((ya_ >> 2) * 512 + bn2 * 64) * 4;
#pragma unroll 1
      for (int s = 3; s < 21; s++) { waitv<8>(); rawbar(); i2(); }
      asm volatile("global_load_dword v126, %0, %1 sc1" ::"v"((u32)lane * 4), "s"(yfb) : "memory", "v126");
#pragma unroll 1
      for (int s = 21; s < 24; s++) { waitv<8>(); rawbar(); i2(); }
      waitv<8>(); rawbar();
      {
        u64 ne;
        asm volatile("v_cmp_ne_u32 %0, 1, v126" : "=s"(ne)::"memory");
        const u32x4 sY = mksrd(ws + WS_Y, 2048u * 1024u);
        const u32 vy = (u32)((bn2 * 64 + 16 * lw + (lane >> 3)) * 4096 + ya_ * 512 + (bm2 & 3) * 64 + ((lane >> 2) & 1) * 256 + (lane & 3) * 16);
        const u32 vx = (u32)((bm2 * 64 + 16 * lw + (lane >> 3)) * 1024 + bn2 * 128 + (((lane & 7) ^ (lane >> 3)) << 4));
        if (ne == 0) {
          dma4<true>(sY, 0u, vy, vy + 32768u - 1024u, sX, 0u, vx, vx + 7168u, 65536u + (u32)lw * 2048u);
          waitv<8>(); rawbar(); waitv<4>(); rawbar();
        } else {
          waitv<4>(); rawbar(); waitv<0>(); rawbar();
          u32 spins = 0, yf;
          do { yf = ld4c_wait(yfb, (u32)lane * 4); } while (!__all(yf == EP) && ++spins < SPIN_MAX);
          dma4<true>(sY, 0u, vy, vy + 32768u - 1024u, sX, 0u, vx, vx + 7168u, 65536u + (u32)lw * 2048u);
        }
      }
      waitv<0>();
      rawbar();
      STAMP(7);
    }
  } else {
    const int G = w >> 2, kch = (w >> 1) & 1, kmh = w & 1;
    const int dch = 2 * tb + kch;
    const bool shk = w == 7;
    __builtin_amdgcn_s_setprio(2);
    int ust = 0;
#define UG_POLL() asm volatile("global_load_dwordx4 v[120:123], %0, %1 sc1" ::"v"((u32)lane * 16), "s"(ws + WS_UFLAG) : "memory", "v120", "v121", "v122", "v123")
    auto bsvc = [&]() {
      if (ust == 0) {
        shk_step();
        if (rel) { asm volatile("v_mov_b32 v120, 0" ::: "v120"); UG_POLL(); ust = 1; }
      } else if (ust == 1) {
        u64 m0_, m1_, m2_, m3_;
        asm volatile("v_cmp_ne_u32 %0, 1, v120\n\tv_cmp_ne_u32 %1, 1, v121\n\tv_cmp_ne_u32 %2, 1, v122\n\tv_cmp_ne_u32 %3, 1, v123"
                     : "=&s"(m0_), "=&s"(m1_), "=&s"(m2_), "=&s"(m3_)::"memory");
        if ((m0_ | m1_ | m2_ | m3_) == 0) {
          u32 vo = (u32)lane * 8192;
#pragma unroll
          for (int j = 0; j < 4; j++)
            glds2<4608, true>(ws + WS_U + (size_t)tb * 32 + (size_t)j * 524288, vo, vo + 16, L_UL + 512 + (u32)j * 1024);
          ust = 2;
        } else UG_POLL();
      }
    };
    u32x4 xr;
    u32 pa, pb, pc_, pd, pe, pf, pl, pk_;
    {
      u32 vo = (u32)((8 * tb + w) * 1024 + lane * 16);
      u32 po = (u32)((dch * 128 + kmh * 64 + lane) * 2);
      u32 lo = (u32)(dch * 2);
      asm volatile(
          "global_load_dwordx4 %0, %9, %12\n\t"
          "global_load_ushort %1, %10, %13\n\t"
          "global_load_ushort %2, %10, %14\n\t"
          "global_load_ushort %3, %10, %15\n\t"
          "global_load_ushort %4, %10, %16\n\t"
          "global_load_ushort %5, %10, %17\n\t"
          "global_load_ushort %6, %10, %18\n\t"
          "global_load_ushort %7, %11, %19\n\t"
          "global_load_ushort %8, %11, %20"
          : "=&v"(xr), "=&v"(pa), "=&v"(pb), "=&v"(pc_), "=&v"(pd), "=&v"(pe), "=&v"(pf), "=&v"(pl), "=&v"(pk_)
          : "v"(vo), "v"(po), "v"(lo), "s"(x), "s"(Are), "s"(Aim), "s"(Bre), "s"(Bim), "s"(Cre), "s"(Cim), "s"(ldt), "s"(Dsk)
          : "memory");
    }
    if (shk) {
      const u32 os = WS_STAT + (u32)(8 * tb + lane) * 8 + 4;
      const u32 oy = WS_YFLAG + (u32)((lane & 1) * 512 + 2 * tb + ((lane >> 1) & 1)) * 4;
      const u32 oh = WS_HFLAG + (u32)((bm * 2 + ((lane >> 1) & 1)) * 32 + bn * 2 + (lane & 1)) * 4;
      u32 off = lane < 8 ? os : (lane < 12 ? oy : ((lane < 16 && isg1) ? oh : WS_UFLAG + (u32)tb * 4));
      if (lane < 17) st4c(ws, off, 0u);
      SHK_POLL();
    }
    const int ech = tid >> 8, eg = tid & 255;
    const u32 pb0 = L_TAB + (u32)ech * 16384;
    u32 ad[12];
    {
      const float lf = (float)(2047 - 8 * eg);
      const u32 cb0 = pb0 - 0x3f00u * 4u;
#pragma unroll
      for (int e = 0; e < 12; e += 2) {
        u32 pk2 = pkbf(fmaxf(lf - (float)e, 0.5f), fmaxf(lf - (float)(e + 1), 0.5f));
        ad[e] = cb0 + ((pk2 & 0xffffu) << 2);
        ad[e + 1] = cb0 + ((pk2 >> 16) << 2);
        asm volatile("" : "+v"(ad[e]), "+v"(ad[e + 1]));
      }
    }
    if (shk) waitv<2>(); else waitv<0>();
    asm volatile("" : "+v"(xr), "+v"(pa), "+v"(pb), "+v"(pc_), "+v"(pd), "+v"(pe), "+v"(pf), "+v"(pl), "+v"(pk_)::"memory");
    STAMP(1);
    {
      float v[8];
#pragma unroll
      for (int j = 0; j < 4; j++) { v[2 * j] = bf2f(xr[j]); v[2 * j + 1] = bfhi(xr[j]); }
      float mean = wave_sum(((v[0] + v[1]) + (v[2] + v[3])) + ((v[4] + v[5]) + (v[6] + v[7]))) * (1.f / 512.f);
      float q = 0.f;
#pragma unroll
      for (int j = 0; j < 8; j++) { float d = v[j] - mean; q = __builtin_fmaf(d, d, q); }
      float var = wave_sum(q) * (1.f / 512.f);
      float mb = rbf(mean), sd = rbf(__builtin_sqrtf(rbf(rbf(var) + 1e-5f)));
      float rs = __builtin_amdgcn_rcpf(sd);
      u32x4 o;
#pragma unroll
      for (int j = 0; j < 4; j++) {
        u32 dd = pkbf(v[2 * j] - mb, v[2 * j + 1] - mb);
        float d0 = bf2f(dd), d1 = bfhi(dd);
        float q0 = d0 * rs, q1 = d1 * rs;
        q0 = __builtin_fmaf(__builtin_fmaf(-q0, sd, d0), rs, q0);
        q1 = __builtin_fmaf(__builtin_fmaf(-q1, sd, d1), rs, q1);
        u32 qq = pkbf(q0, q1);
        o[j] = pkh(bf2f(qq), bfhi(qq));
      }
      ldsst<u32x4>(L_LNS + w * 1088 + lane * 16, o);
      if (lane == 0) ldsst<u32>(L_ST + w * 4, (__float_as_uint(mb) >> 16) | (__float_as_uint(sd) & 0xffff0000u));
    }
    if (shk) { waitv<0>(); shk_first(); }
    bar();
    if (shk) {
      const int li = lane & 15;
      u32 a = L_LNS + (li >> 2) * 1088 + ((lane & 48) + 4 * (li & 3)) * 2;
#pragma unroll
      for (int j = 0; j < 8; j++) {
        u32x2 p0 = ldstr(a + j * 128), p1 = ldstr(a + j * 128 + 4 * 1088);
        st16c(ws + WS_U + (size_t)tb * 8192, (u32)(j * 1024 + lane * 16), (u32x4){p0[0], p0[1], p1[0], p1[1]});
      }
      u32 pay = ldsld<u32>(L_ST + (lane & 7) * 4);
      if (lane < 8) st8c(ws, WS_STAT + (u32)(8 * tb + lane) * 8, (u32x2){pay, EP});
      bsvc();
    }
    STAMP(2);
    const int rr = lane & 15, kg = lane >> 4;
    {
      float dt = rbf(expf(bf2f(pl)));
      float a2 = (dt * bf2f(pa)) * 1.44269504088896341f;
      float bb = dt * bf2f(pb);
      f32x2 W = cmul((f32x2){bf2f(pe), bf2f(pf)}, (f32x2){dt * bf2f(pc_), dt * bf2f(pd)});
      const float C_HI = 0.15915494309189535f, C_LO = (float)(0.15915494309189533577 - (double)0.15915494309189535f);
      float p = bb * C_HI;
      float er = __builtin_fmaf(bb, C_HI, -p);
      float fhi = __uint_as_float(__float_as_uint(p) & 0xfffff000u);
      float flo = (p - fhi) + __builtin_fmaf(bb, C_LO, er);
      float g = 0.f;
      if (__any(a2 > 0.f)) { g = wave_max0(fmaxf(a2, 0.f)); a2 -= g; }
      float wsum = wave_sum(fabsf(W[0]) + fabsf(W[1]));
      float sig_, isig;
      pow2scale(wsum, 13, sig_, isig);
      const f32x2 W0 = {W[0] * sig_, -W[1] * sig_};
      const u32 T = L_TAB + w * 8192;
      const u32 wbase = T + (lane >> 2) * 16 + (lane & 3) * 4;
      const u32 ra = T + rr * 256 + ((kg ^ rr) << 4);
      const float cs = G ? 8.f : 2.f, rs = G ? 128.f : 32.f, rb = G ? 512.f : 0.f;
      f32x2 zc = cpow(a2, -fhi, -flo, cs), zr = cpow(a2, fhi, flo, rs);
      f32x2 zc4 = cpow(a2, -fhi, -flo, 4.f * cs), zr4 = cpow(a2, fhi, flo, 4.f * rs);
      f32x2 zb = cpow(a2, fhi, flo, rb), zx = cpow(a2, fhi, flo, 0.5f * cs);
      f32x2 zc2 = cmul(zc, zc), zr2 = cmul(zr, zr);
      f32x2 c[4], r[4], e0[4], e1[4];
      c[0] = W0; c[1] = cmul(W0, zc); r[0] = zb; r[1] = cmul(zb, zr);
      c[2] = cmul(c[0], zc2); c[3] = cmul(c[1], zc2); r[2] = cmul(r[0], zr2); r[3] = cmul(r[1], zr2);
      SB0;
      if (shk) { waitv<0>(); if (lane == 0) st4c(ws, WS_UFLAG + (u32)tb * 4, EP); bsvc(); }
      SB0;
#pragma unroll
      for (int o = 0; o < 4; o++) {
        if (o > 0) { cmul4(c[0], c[1], c[2], c[3], zc4); cmul4(r[0], r[1], r[2], r[3], zr4); }
        if (o == 0) { e0[0] = r[0]; e0[1] = r[1]; e0[2] = r[2]; e0[3] = r[3]; cmul4(e0[0], e0[1], e0[2], e0[3], zx); }
        if (o == 1) { e1[0] = r[0]; e1[1] = r[1]; e1[2] = r[2]; e1[3] = r[3]; cmul4(e1[0], e1[1], e1[2], e1[3], zx); }
#pragma unroll
        for (int j = 0; j < 4; j++) {
          int e = 4 * o + j;
          u32 a = wbase ^ (u32)(e << 4);
          ldsst<u32>(a + e * 256, pkh(r[j][0], r[j][1]));
          ldsst<u32>(a + e * 256 + 4096, pkh(c[j][0], c[j][1]));
        }
        if (shk) bsvc();
      }
      f16x8 af[4], bf[4];
#pragma unroll
      for (int s = 0; s < 4; s++) { af[s] = ldsld<f16x8>(ra ^ (u32)(s << 6)); bf[s] = ldsld<f16x8>((ra ^ (u32)(s << 6)) + 4096); }
#pragma unroll
      for (int j = 0; j < 4; j++) {
        ldsst<u32>((wbase ^ (u32)(j << 4)) + j * 256, pkh(e0[j][0], e0[j][1]));
        ldsst<u32>((wbase ^ (u32)((4 + j) << 4)) + (4 + j) * 256, pkh(e1[j][0], e1[j][1]));
      }
      f16x8 ag[4];
#pragma unroll
      for (int s = 0; s < 4; s++) ag[s] = ldsld<f16x8>(ra ^ (u32)(s << 6));
      if (shk) bsvc();
      bar();
      if (shk) bsvc();
      f32x4 d0 = {0.f, 0.f, 0.f, 0.f}, d1 = {0.f, 0.f, 0.f, 0.f};
#pragma unroll
      for (int s = 0; s < 4; s++) d0 = __builtin_amdgcn_mfma_f32_16x16x32_f16(af[s], bf[s], d0, 0, 0, 0);
#pragma unroll
      for (int s = 0; s < 4; s++) d1 = __builtin_amdgcn_mfma_f32_16x16x32_f16(ag[s], bf[s], d1, 0, 0, 0);
      const u32 PB = L_TAB + (u32)(w & 3) * 8192 - 0x3f00u * 4u;
      const float l00 = rb + rs * (float)(4 * kg) + cs * (float)rr;
#pragma unroll
      for (int e = 0; e < 4; e++) {
        float l0 = l00 + rs * (float)e, l1 = l0 + 0.5f * cs;
        float s0 = isig, s1 = isig;
        if (g > 0.f) { s0 *= __builtin_amdgcn_exp2f(g * l0); s1 *= __builtin_amdgcn_exp2f(g * l1); }
        l0 = fmaxf(l0, 0.5f);
        l0 = l0 > 2048.f ? 4096.f : l0;
        l1 = l1 < 128.f * cs ? l1 : 4096.f;
        ldsst<float>(PB + ((__float_as_uint(l0) >> 16) << 2), d0[e] * s0);
        ldsst<float>(PB + ((__float_as_uint(l1) >> 16) << 2), d1[e] * s1);
      }
      if (lane == 0 && G == 0) ldsst<float>(T + 7168, wsum * __builtin_amdgcn_exp2f(g * 2048.f));
    }
    if (shk) bsvc();
    STAMP(3);
    bar();
    if (shk) bsvc();
    float kinv;
    {
      float bnd = ldsld<float>(pb0 + 7168) + ldsld<float>(pb0 + 8192 + 7168);
      float bnd2 = ldsld<float>(L_TAB + kch * 16384 + 7168) + ldsld<float>(L_TAB + kch * 16384 + 8192 + 7168);
      float v[11];
#pragma unroll
      for (int e = 0; e < 11; e++) v[e] = ldsld<float>(ad[e]) + ldsld<float>(ad[e] + 8192);
      float ks, ki, ks2;
      pow2scale(bnd, 14, ks, ki);
      pow2scale(bnd2, 14, ks2, kinv);
      u32 PK[5], QK[5];
#pragma unroll
      for (int e = 0; e < 11; e++) v[e] *= (e >= 8 && eg == 255) ? 0.f : ks;
#pragma unroll
      for (int j = 0; j < 5; j++) { PK[j] = pkh(v[2 * j], v[2 * j + 1]); QK[j] = pkh(v[2 * j + 1], v[2 * j + 2]); }
      u32 a = L_KCP + ech * 16640 + eg * 16;
      ldsst<u32x4>(a, (u32x4){PK[0], PK[1], PK[2], PK[3]});
      ldsst<u32x4>(a + 4160, (u32x4){QK[0], QK[1], QK[2], QK[3]});
      ldsst<u32x4>(a + 8320, (u32x4){PK[1], PK[2], PK[3], PK[4]});
      ldsst<u32x4>(a + 12480, (u32x4){QK[1], QK[2], QK[3], QK[4]});
      if (tid < 16) ldsst<u32x4>(L_KCP + (tid >> 1) * 4160 + (256 + (tid & 1)) * 16, (u32x4){0, 0, 0, 0});
      else if (tid >= 64 && tid < 128) ldsst<u32x4>(L_UL + ((tid >> 5) & 1) * 4608 + (tid & 31) * 16, (u32x4){0, 0, 0, 0});
    }
    STAMP(4);
    if (shk) {
      u32 spins = 0;
      while (ust < 2 && ++spins < SPIN_MAX) { waitv<0>(); bsvc(); }
      STAMP(7);
      waitv<0>();
    }
    __builtin_amdgcn_s_setprio(2);
    bar();
    STAMP(5);
    if (w < 4) {
      const int n = rr;
      f32x4 acc[8];
#pragma unroll
      for (int a = 0; a < 8; a++) acc[a] = (f32x4){0.f, 0.f, 0.f, 0.f};
      const int cc = (3 - n) & 3;
      u32 ub = L_UL + kch * 4608 + (u32)((256 + 16 * (n - 8 * kmh) - 16 + 8 * kg) * 2);
      u32 ab = L_KCP + kch * 16640 + cc * 4160 + (u32)((2031 - n + 8 * kg - cc - 1792 - 128 * kmh) * 2);
      f16x8 Bq[8];
      u32x2 Al[4], Ah[4];
#pragma unroll
      for (int q = 0; q < 8; q++) Bq[q] = ldsld<f16x8>(ub + q * 512);
#pragma unroll
      for (int i = 0; i < 4; i++) { Al[i] = ldsldv<u32x2>(ab + (7 - i) * 512); Ah[i] = ldsldv<u32x2>(ab + (7 - i) * 512 + 8); }
#pragma unroll 1
      for (int it = 0; it < 4; it++) {
#pragma unroll
        for (int i = 0; i < 8; i++) {
          const int in = (i + 4) & 7;
          u32 a = ab + (7 - in) * 512 - (i >= 4 ? 64 : 0);
          f16x8 A = __builtin_bit_cast(f16x8, (u32x4){Al[i & 3][0], Al[i & 3][1], Ah[i & 3][0], Ah[i & 3][1]});
#pragma unroll
          for (int a2 = 7; a2 >= i; a2--) acc[a2] = __builtin_amdgcn_mfma_f32_16x16x32_f16(A, Bq[a2 - i], acc[a2], 0, 0, 0);
          SB0;
          Al[i & 3] = ldsldv<u32x2>(a); Ah[i & 3] = ldsldv<u32x2>(a + 8);
          Bq[7 - i] = ldsld<f16x8>(ub - 64 + (7 - i) * 512);
          SB0;
        }
        ub -= 64; ab -= 64;
      }
      STAMP(6);
      if (kmh) {
#pragma unroll
        for (int a = 0; a < 4; a++) { f32x4 t = acc[a]; acc[a] = acc[4 + a]; acc[4 + a] = t; asm volatile("" : "+v"(acc[a]), "+v"(acc[4 + a])); }
      }
#pragma unroll
      for (int a = 0; a < 4; a++) ldsst<f32x4>(L_RED + (u32)(w * 4096 + a * 1024 + lane * 16), acc[4 + a]);
      bar();
      const float dsk = bf2f(pk_);
      char* yp = ws + WS_Y + (size_t)dch * 4096 + kmh * 2048;
#pragma unroll
      for (int pr = 0; pr < 2; pr++) {
        u32 pk[2][2];
#pragma unroll
        for (int j = 0; j < 2; j++) {
          const int a = 2 * pr + j;
          f32x4 yy = acc[a] + ldsld<f32x4>(L_RED + (u32)((w ^ 1) * 4096 + a * 1024 + lane * 16));
          f16x4 uu = ldsld<f16x4>(L_UL + kch * 4608 + 512 + (u32)((256 * (4 * kmh + a) + 16 * n + 4 * kg) * 2));
          u32 y01 = pkbf(yy[0] * kinv, yy[1] * kinv), y23 = pkbf(yy[2] * kinv, yy[3] * kinv);
          u32 s01 = pkbf((float)uu[0] * dsk, (float)uu[1] * dsk), s23 = pkbf((float)uu[2] * dsk, (float)uu[3] * dsk);
          pk[j][0] = pkbf(bf2f(y01) + bf2f(s01), bfhi(y01) + bfhi(s01));
          pk[j][1] = pkbf(bf2f(y23) + bf2f(s23), bfhi(y23) + bfhi(s23));
        }
        auto s0 = __builtin_amdgcn_permlane16_swap(pk[0][0], pk[1][0], false, false);
        auto s1 = __builtin_amdgcn_permlane16_swap(pk[0][1], pk[1][1], false, false);
        st16c(yp, (u32)((2 * pr + (kg & 1)) * 512 + (kg >> 1) * 256 + n * 16), (u32x4){s0[0], s1[0], s0[1], s1[1]});
      }
      waitv<0>();
      if (lane == 0) st4c(ws, WS_YFLAG + (u32)(kmh * 512 + dch) * 4, EP);
      STAMP(8);
    }
  }
  STAMP_OUT();
}

extern "C" void kernel_launch(void* const* d_in, const int* in_sizes, int n_in, void* d_out, int out_size, void* d_ws,
                              size_t ws_size, hipStream_t stream) {
  fused_kernel<<<512, 512, 0, stream>>>((const u16*)d_in[0], (const u16*)d_in[1], (const u16*)d_in[2], (const u16*)d_in[3],
                                        (const u16*)d_in[4], (const u16*)d_in[5], (const u16*)d_in[6], (const u16*)d_in[7],
                                        (const u16*)d_in[8], (const u16*)d_in[10], (const u16*)d_in[11], (const u16*)d_in[12],
                                        (u16*)d_out, (char*)d_ws);
}
```

```cpp
#include <hip/hip_runtime.h>
#include <stdint.h>
#include <type_traits>

typedef unsigned short u16;
typedef unsigned int u32;
typedef unsigned long long u64;
typedef __attribute__((ext_vector_type(8))) __bf16 bf16x8;
typedef __attribute__((ext_vector_type(2))) __bf16 bf16x2;
typedef __attribute__((ext_vector_type(8))) _Float16 f16x8;
typedef __attribute__((ext_vector_type(4))) _Float16 f16x4;
typedef __attribute__((ext_vector_type(2))) _Float16 f16x2;
typedef __attribute__((ext_vector_type(2))) float f32x2;
typedef __attribute__((ext_vector_type(4))) float f32x4;
typedef __attribute__((ext_vector_type(16))) float f32x16;
typedef __attribute__((ext_vector_type(4))) u32 u32x4;
typedef __attribute__((ext_vector_type(2))) u32 u32x2;
typedef __attribute__((ext_vector_type(4))) short s16x4;
#define AS3 __attribute__((address_space(3)))
#define DEVI __device__ __forceinline__
template <int N> using IC = std::integral_constant<int, N>;

constexpr u32 WS_STAT = 0;
constexpr u32 WS_CS = 16384;
constexpr u32 WS_UFLAG = 30720;
constexpr u32 WS_YFLAG = 31744;
constexpr u32 WS_HFLAG = 35840;
constexpr u32 WS_R = 40960;
constexpr u32 WS_A = 45056;
constexpr u32 WS_N = 49152;
constexpr size_t WS_U = 1u << 20, WS_Y = 4u << 20, WS_H = 8u << 20;
constexpr u32 LA_XA = 16384, LA_WA = 17408, LA_RS = 18432, LA_ZERO = 18944;
constexpr u32 L_TAB = 0;
constexpr u32 L_KCP = 32768;
constexpr u32 L_UL = 66048;
constexpr u32 L_LNS = 66048;
constexpr u32 L_ST = 75264;
constexpr u32 L_RED = 0;
constexpr u32 SPIN_MAX = 1u << 15;
constexpr u32 EP = 1u;
constexpr int LEADER = 248;
constexpr int PMASK = 0x7ffe;

template <typename T> DEVI T ldsld(u32 a) { return *(AS3 T*)(uintptr_t)a; }
template <typename T> DEVI T ldsldv(u32 a) { return *(volatile AS3 T*)(uintptr_t)a; }
template <typename T> DEVI void ldsst(u32 a, T v) { *(AS3 T*)(uintptr_t)a = v; }
DEVI u32x2 ldstr(u32 a) { return __builtin_bit_cast(u32x2, __builtin_amdgcn_ds_read_tr16_b64_v4i16((AS3 s16x4*)(uintptr_t)a)); }
DEVI float bf2f(u32 v) { return __uint_as_float(v << 16); }
DEVI float bfhi(u32 v) { return __uint_as_float(v & 0xffff0000u); }
DEVI float rbf(float f) { return (float)(__bf16)f; }
DEVI u32 pkbf(float a, float b) { bf16x2 v = {(__bf16)a, (__bf16)b}; return __builtin_bit_cast(u32, v); }
DEVI u32 pkh(float a, float b) { f16x2 v = {(_Float16)a, (_Float16)b}; return __builtin_bit_cast(u32, v); }

template <int STEP, bool COH>
DEVI void glds2(const void* sbase, u32 v0, u32 v1, u32 lds0) {
  u32 keep;
  if (COH)
    asm volatile("s_mov_b32 %0, m0\n\ts_mov_b32 m0, %4\n\ts_nop 0\n\tglobal_load_lds_dwordx4 %1, %3 sc1\n\t"
                 "s_add_u32 m0, m0, %5\n\ts_nop 0\n\tglobal_load_lds_dwordx4 %2, %3 sc1\n\ts_mov_b32 m0, %0"
                 : "=&s"(keep) : "v"(v0), "v"(v1), "s"(sbase), "s"(lds0), "n"(STEP) : "memory", "scc");
  else
    asm volatile("s_mov_b32 %0, m0\n\ts_mov_b32 m0, %4\n\ts_nop 0\n\tglobal_load_lds_dwordx4 %1, %3\n\t"
                 "s_add_u32 m0, m0, %5\n\ts_nop 0\n\tglobal_load_lds_dwordx4 %2, %3\n\ts_mov_b32 m0, %0"
                 : "=&s"(keep) : "v"(v0), "v"(v1), "s"(sbase), "s"(lds0), "n"(STEP) : "memory", "scc");
}
DEVI u32x4 mksrd(const void* p, u32 bytes) {
  u64 a = (u64)p;
  return (u32x4){(u32)a, (u32)(a >> 32) & 0xffffu, bytes, 0x00020000u};
}
template <bool COHA>
DEVI void dma4(u32x4 sA, u32 soA, u32 vA0, u32 vA1, u32x4 sB, u32 soB, u32 vB0, u32 vB1, u32 ldsA) {
  if (COHA)
    asm volatile("s_mov_b32 m0, %8\n\ts_nop 0\n\tbuffer_load_dwordx4 %2, %0, %1 offen sc1 lds\n\t"
                 "buffer_load_dwordx4 %3, %0, %1 offen offset:1024 sc1 lds\n\ts_add_u32 m0, m0, 0x2000\n\ts_nop 0\n\t"
                 "buffer_load_dwordx4 %6, %4, %5 offen lds\n\tbuffer_load_dwordx4 %7, %4, %5 offen offset:1024 lds"
                 ::"s"(sA), "s"(soA), "v"(vA0), "v"(vA1), "s"(sB), "s"(soB), "v"(vB0), "v"(vB1), "s"(ldsA) : "memory", "scc");
  else
    asm volatile("s_mov_b32 m0, %8\n\ts_nop 0\n\tbuffer_load_dwordx4 %2, %0, %1 offen lds\n\t"
                 "buffer_load_dwordx4 %3, %0, %1 offen offset:1024 lds\n\ts_add_u32 m0, m0, 0x2000\n\ts_nop 0\n\t"
                 "buffer_load_dwordx4 %6, %4, %5 offen lds\n\tbuffer_load_dwordx4 %7, %4, %5 offen offset:1024 lds"
                 ::"s"(sA), "s"(soA), "v"(vA0), "v"(vA1), "s"(sB), "s"(soB), "v"(vB0), "v"(vB1), "s"(ldsA) : "memory", "scc");
}
template <bool COH>
DEVI void dma2(u32x4 sA, u32 soA, u32 vA0, u32 vA1, u32 ldsA) {
  if (COH)
    asm volatile("s_mov_b32 m0, %4\n\ts_nop 0\n\tbuffer_load_dwordx4 %2, %0, %1 offen sc1 lds\n\t"
                 "buffer_load_dwordx4 %3, %0, %1 offen offset:1024 sc1 lds" ::"s"(sA), "s"(soA), "v"(vA0), "v"(vA1), "s"(ldsA) : "memory");
  else
    asm volatile("s_mov_b32 m0, %4\n\ts_nop 0\n\tbuffer_load_dwordx4 %2, %0, %1 offen lds\n\t"
                 "buffer_load_dwordx4 %3, %0, %1 offen offset:1024 lds" ::"s"(sA), "s"(soA), "v"(vA0), "v"(vA1), "s"(ldsA) : "memory");
}
DEVI void rawbar() { asm volatile("" ::: "memory"); __builtin_amdgcn_s_barrier(); asm volatile("" ::: "memory"); }
DEVI void st16c(void* sbase, u32 voff, u32x4 v) {
  asm volatile("global_store_dwordx4 %0, %1, %2 sc1\n\ts_nop 1" ::"v"(voff), "v"(v), "s"(sbase) : "memory");
}
DEVI void st16(void* sbase, u32 voff, u32x4 v) {
  asm volatile("global_store_dwordx4 %0, %1, %2\n\ts_nop 1" ::"v"(voff), "v"(v), "s"(sbase) : "memory");
}
DEVI void st8c(void* sbase, u32 voff, u32x2 v) {
  asm volatile("global_store_dwordx2 %0, %1, %2 sc1\n\ts_nop 1" ::"v"(voff), "v"(v), "s"(sbase) : "memory");
}
DEVI void st4c(void* sbase, u32 voff, u32 v) {
  asm volatile("global_store_dword %0, %1, %2 sc1\n\ts_nop 1" ::"v"(voff), "v"(v), "s"(sbase) : "memory");
}
DEVI u32 ld4c_wait(const void* sbase, u32 voff) {
  u32 r;
  asm volatile("global_load_dword %0, %1, %2 sc1\n\ts_waitcnt vmcnt(0)" : "=&v"(r) : "v"(voff), "s"(sbase) : "memory");
  return r;
}
template <int N> DEVI void waitv() { asm volatile("s_waitcnt vmcnt(%0)" ::"n"(N) : "memory"); }
DEVI void bar() {
  asm volatile("s_waitcnt lgkmcnt(0)" ::: "memory");
  __builtin_amdgcn_s_barrier();
  asm volatile("" ::: "memory");
}
template <int CTRL, int RM> DEVI float dppf(float old, float v) {
  return __int_as_float(__builtin_amdgcn_update_dpp(__float_as_int(old), __float_as_int(v), CTRL, RM, 0xf, false));
}
DEVI float wave_sum(float v) {
  v += dppf<0x111, 0xf>(0.f, v); v += dppf<0x112, 0xf>(0.f, v); v += dppf<0x114, 0xf>(0.f, v); v += dppf<0x118, 0xf>(0.f, v);
  v += dppf<0x142, 0xa>(0.f, v); v += dppf<0x143, 0xc>(0.f, v);
  return __int_as_float(__builtin_amdgcn_readlane(__float_as_int(v), 63));
}
DEVI float wave_max0(float v) {
  v = fmaxf(v, dppf<0x111, 0xf>(0.f, v)); v = fmaxf(v, dppf<0x112, 0xf>(0.f, v)); v = fmaxf(v, dppf<0x114, 0xf>(0.f, v));
  v = fmaxf(v, dppf<0x118, 0xf>(0.f, v)); v = fmaxf(v, dppf<0x142, 0xa>(0.f, v)); v = fmaxf(v, dppf<0x143, 0xc>(0.f, v));
  return __int_as_float(__builtin_amdgcn_readlane(__float_as_int(v), 63));
}
#define CM_MUL(t, a, b) "v_pk_mul_f32 " t ", " a ", " b " op_sel_hi:[0,1]\n\t"
#define CM_FMA(r, a, b, t) "v_pk_fma_f32 " r ", " a ", " b ", " t " op_sel:[1,1,0] op_sel_hi:[1,0,1] neg_lo:[1,0,0]\n\t"
DEVI f32x2 cmul(f32x2 a, f32x2 b) {
  f32x2 t, r;
  asm(CM_MUL("%0", "%1", "%2") : "=v"(t) : "v"(a), "v"(b));
  asm(CM_FMA("%0", "%1", "%2", "%3") : "=v"(r) : "v"(a), "v"(b), "v"(t));
  return r;
}
DEVI void cmul4(f32x2& a0, f32x2& a1, f32x2& a2, f32x2& a3, f32x2 z) {
  f32x2 t0, t1, t2, t3;
  asm(CM_MUL("%0", "%4", "%8") CM_MUL("%1", "%5", "%8") CM_MUL("%2", "%6", "%8") CM_MUL("%3", "%7", "%8")
      CM_FMA("%0", "%4", "%8", "%0") CM_FMA("%1", "%5", "%8", "%1") CM_FMA("%2", "%6", "%8", "%2") CM_FMA("%3", "%7", "%8", "%3")
      : "=&v"(t0), "=&v"(t1), "=&v"(t2), "=&v"(t3) : "v"(a0), "v"(a1), "v"(a2), "v"(a3), "v"(z));
  a0 = t0; a1 = t1; a2 = t2; a3 = t3;
}
DEVI f32x2 cpow(float a2, float fhi, float flo, float R) {
  float m = __builtin_amdgcn_exp2f(a2 * R);
  float ph = __builtin_fmaf(flo, R, __builtin_amdgcn_fractf(fhi * R));
  return (f32x2){m * __builtin_amdgcn_cosf(ph), m * __builtin_amdgcn_sinf(ph)};
}
DEVI void pow2scale(float mx, int target, float& s, float& inv) {
  int ex = (int)((__float_as_uint(mx) >> 23) & 0xff);
  int se = 254 + target - ex;
  se = se < 2 ? 2 : (se > 252 ? 252 : se);
  s = __uint_as_float((u32)se << 23);
  inv = __uint_as_float((u32)(254 - se) << 23);
}

DEVI u32 min4(u32x4 f) { return __builtin_amdgcn_readfirstlane(min(min(f[0], f[1]), min(f[2], f[3]))); }

__global__ __launch_bounds__(512, 4) void fused_kernel(
    const u16* __restrict__ x, const u16* __restrict__ Are, const u16* __restrict__ Aim, const u16* __restrict__ Bre,
    const u16* __restrict__ Bim, const u16* __restrict__ Cre, const u16* __restrict__ Cim, const u16* __restrict__ ldt,
    const u16* __restrict__ Dsk, const u16* __restrict__ w1, const u16* __restrict__ b1, const u16* __restrict__ w2,
    u16* __restrict__ out, char* __restrict__ ws) {
  __shared__ __attribute__((aligned(16))) char sm[81920];
  asm volatile("" ::"v"(sm) : "memory");
  const int tid = threadIdx.x, lane = tid & 63;
  const int w = __builtin_amdgcn_readfirstlane(tid >> 6);
  const int gb = blockIdx.x, b = gb & 255, xcd = b & 7, bi = b >> 3;
  const bool isg1 = bi < 28;
  const int bm = 2 * xcd + (bi & 1), bn = bi >> 1;
  const int tb = 32 * xcd + bi;
#define SB0 __builtin_amdgcn_sched_barrier(0)
  const u32 ao = lane == 0 ? WS_N + (u32)(gb & 31) * 128u : WS_A + (u32)gb * 4;
#define SHK_POLL() asm volatile("global_load_dword v127, %0, %1 sc1" ::"v"(ao), "s"(ws) : "memory", "v127")
  u32 rb_ = 0, nsent = 0;
  bool rel = false;
  auto shk_first = [&]() {
    u32 n, a;
    asm volatile("v_readlane_b32 %0, v127, 0\n\tv_readlane_b32 %1, v127, 1" : "=s"(n), "=s"(a)::"memory");
    rb_ = a + 1u; nsent = n;
    if (lane == 0) st8c(ws, WS_R + (u32)gb * 8, (u32x2){rb_, n});
    SHK_POLL();
  };
  auto shk_step = [&]() {
    u32 n, a;
    asm volatile("v_readlane_b32 %0, v127, 0\n\tv_readlane_b32 %1, v127, 1" : "=s"(n), "=s"(a)::"memory");
    if (a == rb_) rel = true;
    else if (n != nsent) { nsent = n; if (lane == 0) st8c(ws, WS_R + (u32)gb * 8, (u32x2){rb_, n}); }
    SHK_POLL();
  };
  auto shk_finish = [&]() {
    u32 spins = 0;
    while (!rel && ++spins < SPIN_MAX) { waitv<0>(); shk_step(); }
    waitv<0>();
  };
  if (gb < 256) {
    const int bm2 = 4 * xcd + (bi & 3), bn2 = bi >> 2;
    const int ci = (7 - xcd) * 4 + (bi - 28);
    if (w < 4) {
      __builtin_amdgcn_s_setprio(3);
      const int hh = lane >> 5, blk = (lane >> 4) & 1, q4 = (lane & 15) >> 2, p4 = lane & 3;
      u32 sb = 0;
      if (isg1) {
        const int wf = w & 1, wt = w >> 1;
        const int tl = wt * 64 + (lane & 31);
        const u32 xa0 = (u32)(tl * 64 + ((hh ^ ((tl >> 2) & 3)) << 4));
        const u32 wa0 = (u32)(8192 + (8 * hh + q4) * 256 +
                              (((8 * wf + 4 * (p4 & 1) + blk) ^ (((q4 & 1) << 1) | ((q4 & 2) << 2))) << 4) + 8 * (p4 >> 1));
        f32x16 acc[2][2];
#pragma unroll
        for (int i = 0; i < 16; i++) { acc[0][0][i] = 0.f; acc[0][1][i] = 0.f; acc[1][0][i] = 0.f; acc[1][1][i] = 0.f; }
        bf16x8 FX[3][2], FW[3][2];
        auto half = [&](auto C_, auto R_, auto KS_) {
          constexpr int C = decltype(C_)::value, R = decltype(R_)::value, ks = decltype(KS_)::value;
          const u32 ax = sb + (xa0 ^ (u32)(ks << 5)), aw0 = sb + wa0 + ks * 4096, aw1 = sb + (wa0 ^ 32u) + ks * 4096;
          u32x2 a0, a1, a2, a3;
          SB0; acc[0][0] = __builtin_amdgcn_mfma_f32_32x32x16_bf16(FW[C][0], FX[C][0], acc[0][0], 0, 0, 0); SB0;
          FX[R][0] = ldsld<bf16x8>(ax); a0 = ldstr(aw0);
          SB0; acc[0][1] = __builtin_amdgcn_mfma_f32_32x32x16_bf16(FW[C][0], FX[C][1], acc[0][1], 0, 0, 0); SB0;
          a1 = ldstr(aw0 + 1024); FX[R][1] = ldsld<bf16x8>(ax + 2048);
          SB0; acc[1][0] = __builtin_amdgcn_mfma_f32_32x32x16_bf16(FW[C][1], FX[C][0], acc[1][0], 0, 0, 0); SB0;
          a2 = ldstr(aw1); a3 = ldstr(aw1 + 1024);
          SB0; acc[1][1] = __builtin_amdgcn_mfma_f32_32x32x16_bf16(FW[C][1], FX[C][1], acc[1][1], 0, 0, 0); SB0;
          FW[R][0] = __builtin_bit_cast(bf16x8, (u32x4){a0[0], a0[1], a1[0], a1[1]});
          FW[R][1] = __builtin_bit_cast(bf16x8, (u32x4){a2[0], a2[1], a3[0], a3[1]});
        };
        auto step = [&](auto A_, auto B_, auto C_) {
          rawbar();
          sb = sb == 65536u ? 0u : sb + 16384u;
          half(A_, C_, IC<0>{}); half(B_, A_, IC<1>{});
        };
        rawbar();
#pragma unroll
        for (int ks = 0; ks < 2; ks++)
#pragma unroll
          for (int j = 0; j < 2; j++) {
            FX[ks][j] = ldsld<bf16x8>((xa0 ^ (u32)(ks << 5)) + j * 2048);
            u32 a = (wa0 ^ (u32)(j << 5)) + ks * 4096;
            u32x2 a0 = ldstr(a), a1 = ldstr(a + 1024);
            FW[ks][j] = __builtin_bit_cast(bf16x8, (u32x4){a0[0], a0[1], a1[0], a1[1]});
          }
#pragma unroll 1
        for (int s = 0; s < 15; s += 3) { step(IC<0>{}, IC<1>{}, IC<2>{}); step(IC<2>{}, IC<0>{}, IC<1>{}); step(IC<1>{}, IC<2>{}, IC<0>{}); }
#pragma unroll
        for (int ks = 0; ks < 2; ks++)
#pragma unroll
          for (int tau = 0; tau < 2; tau++)
#pragma unroll
            for (int j = 0; j < 2; j++)
              acc[tau][j] = __builtin_amdgcn_mfma_f32_32x32x16_bf16(FW[ks][tau], FX[ks][j], acc[tau][j], 0, 0, 0);
        bar();
        int l2 = lane;
        asm volatile("" : "+v"(l2));
        const int hh2 = l2 >> 5, tl2 = wt * 64 + (l2 & 31);
        {
          const int li = l2 & 15;
          const u32 fo = (u32)(wf * 64 + 32 * ((li >> 2) & 1) + 8 * ((l2 >> 4) & 1) + 4 * (li >> 3) + (li & 3));
          bf16x8 xa[2], wa[2];
#pragma unroll
          for (int j = 0; j < 2; j++) { u32x2 t = ldsld<u32x2>(hh2 ? LA_ZERO : LA_XA + (tl2 + 32 * j) * 8); xa[j] = __builtin_bit_cast(bf16x8, (u32x4){t[0], t[1], 0u, 0u}); }
#pragma unroll
          for (int tau = 0; tau < 2; tau++) { u32x2 t = ldsld<u32x2>(hh2 ? LA_ZERO : LA_WA + (fo + 16 * tau) * 8); wa[tau] = __builtin_bit_cast(bf16x8, (u32x4){t[0], t[1], 0u, 0u}); }
#pragma unroll
          for (int tau = 0; tau < 2; tau++)
#pragma unroll
            for (int j = 0; j < 2; j++) acc[tau][j] = __builtin_amdgcn_mfma_f32_32x32x16_bf16(wa[tau], xa[j], acc[tau][j], 0, 0, 0);
        }
        char* hp = ws + WS_H + (size_t)(bm * 2 + wt) * 229376 + (bn * 16 + wf * 8) * 1024;
#pragma unroll
        for (int j = 0; j < 2; j++) {
          const float r = ldsld<float>(LA_RS + (tl2 + 32 * j) * 4);
          const float c1 = r * 1.44269504088896341f, m2r = -2.f * r;
          const f32x2 c1v = {c1, c1}, rv = {r, r}, m2v = {m2r, m2r}, one = {1.f, 1.f};
#pragma unroll
          for (int tau = 0; tau < 2; tau++) {
#pragma unroll
            for (int hf = 0; hf < 2; hf++) {
              f32x2 av[4], t[4];
#pragma unroll
              for (int jj = 0; jj < 4; jj++) { av[jj] = (f32x2){acc[tau][j][8 * hf + 2 * jj], acc[tau][j][8 * hf + 2 * jj + 1]}; t[jj] = av[jj] * c1v; }
#pragma unroll
              for (int jj = 0; jj < 4; jj++) t[jj] = (f32x2){__builtin_amdgcn_exp2f(t[jj][0]), __builtin_amdgcn_exp2f(t[jj][1])};
#pragma unroll
              for (int jj = 0; jj < 4; jj++) t[jj] = t[jj] + one;
#pragma unroll
              for (int jj = 0; jj < 4; jj++) t[jj] = __builtin_elementwise_fma(t[jj], t[jj], one);
#pragma unroll
              for (int jj = 0; jj < 4; jj++) t[jj] = (f32x2){__builtin_amdgcn_rcpf(t[jj][0]), __builtin_amdgcn_rcpf(t[jj][1])};
#pragma unroll
              for (int jj = 0; jj < 4; jj++) t[jj] = __builtin_elementwise_fma(t[jj], m2v, rv);
              u32x4 o;
#pragma unroll
              for (int jj = 0; jj < 4; jj++) { f32x2 ov = av[jj] * t[jj]; o[jj] = pkbf(ov[0], ov[1]); }
              st16c(hp, (u32)((4 * hh2 + 2 * tau + hf) * 1024 + ((l2 & 31) + 32 * j) * 16), o);
            }
          }
        }
        waitv<0>();
        if (l2 == 0) st4c(ws, WS_HFLAG + (u32)((bm * 2 + wt) * 32 + bn * 2 + wf) * 4, EP);
        bar();
      } else {
        const bool lead = b == LEADER && w == 1;
        u32 cand = 0;
        u32x4 r0, r1, r2, r3;
#define LEAD_LOAD() asm volatile("global_load_dwordx4 %0, %4, %5 sc1\n\tglobal_load_dwordx4 %1, %4, %5 offset:16 sc1\n\t" \
                       "global_load_dwordx4 %2, %4, %5 offset:32 sc1\n\tglobal_load_dwordx4 %3, %4, %5 offset:48 sc1\n\ts_waitcnt vmcnt(0)" \
                       : "=&v"(r0), "=&v"(r1), "=&v"(r2), "=&v"(r3) : "v"((u32)lane * 64), "s"(ws + WS_R) : "memory")
        if (lead) {
          u32 nold;
          asm volatile("global_load_dword %0, %1, %2 sc1" : "=&v"(nold) : "v"(WS_N + (u32)(lane & 31) * 128u), "s"(ws) : "memory");
          LEAD_LOAD();
          asm volatile("" : "+v"(nold)::"memory");
          cand = __builtin_amdgcn_readfirstlane(nold) + 1u;
          while (__any(nold == cand || r0[1] == cand || r0[3] == cand || r1[1] == cand || r1[3] == cand || r2[1] == cand ||
                       r2[3] == cand || r3[1] == cand || r3[3] == cand)) cand++;
          if (lane < 32) st4c(ws, WS_N + (u32)lane * 128u, cand);
        }
        rawbar();
        if (ci < 28) {
          float cs[8];
#pragma unroll
          for (int e = 0; e < 8; e++) cs[e] = 0.f;
#pragma unroll 4
          for (int j = 0; j < 16; j++) {
            u32x4 cw = ldsld<u32x4>((u32)(j * 4096 + tid * 16));
#pragma unroll
            for (int e = 0; e < 4; e++) { cs[2 * e] += bf2f(cw[e]); cs[2 * e + 1] += bfhi(cw[e]); }
          }
          ldsst<f32x4>((u32)(65536 + tid * 32), (f32x4){cs[0], cs[1], cs[2], cs[3]});
          ldsst<f32x4>((u32)(65536 + tid * 32 + 16), (f32x4){cs[4], cs[5], cs[6], cs[7]});
        }
        bar();
        if (ci < 28 && w == 0) {
          float s = 0.f;
#pragma unroll 8
          for (int r = 0; r < 32; r++) s += ldsld<float>((u32)(65536 + r * 256 + lane * 4));
          st8c(ws, WS_CS + (u32)(ci * 512 + lane * 8), (u32x2){__float_as_uint(s), EP});
        }
        if (lead) {
          u32 spins = 0;
          for (;;) {
            LEAD_LOAD();
            bool ok = r0[1] == cand && r0[3] == cand && r1[1] == cand && r1[3] == cand && r2[1] == cand && r2[3] == cand &&
                      r3[1] == cand && r3[3] == cand;
            if (__all(ok) || ++spins > SPIN_MAX) break;
          }
          st16c(ws + WS_A, (u32)lane * 32, (u32x4){r0[0], r0[2], r1[0], r1[2]});
          st16c(ws + WS_A, (u32)lane * 32 + 16, (u32x4){r2[0], r2[2], r3[0], r3[2]});
        }
      }
      int l3 = lane;
      asm volatile("" : "+v"(l3));
      const int h3 = l3 >> 5, blk3 = (l3 >> 4) & 1, q3 = (l3 & 15) >> 2, p3 = l3 & 3;
      sb = 16384u;
      const int wd = w & 1, wt2 = w >> 1;
      const int tl = wt2 * 32 + (l3 & 31);
      const u32 ha0 = (u32)(h3 * 1024 + tl * 16);
      const u32 wa0 = (u32)(8192 + (8 * h3 + q3) * 128 + (((4 * wd + 2 * (p3 & 1) + blk3) ^ ((q3 & 2) << 1)) << 4) + 8 * (p3 >> 1));
      const int dl = wd * 32 + 16 * h3;
      f32x16 acc;
#pragma unroll
      for (int i = 0; i < 16; i++) acc[i] = 0.f;
      bf16x8 FH[2][4], FW[2][4];
      auto rdf2 = [&](auto P_, int ks) {
        constexpr int P = decltype(P_)::value;
        FH[P][ks] = ldsld<bf16x8>(sb + ha0 + ks * 2048);
        u32 a = sb + wa0 + ks * 2048;
        u32x2 a0 = ldstr(a), a1 = ldstr(a + 512);
        FW[P][ks] = __builtin_bit_cast(bf16x8, (u32x4){a0[0], a0[1], a1[0], a1[1]});
      };
      auto step2 = [&](auto P_) {
        constexpr int P = decltype(P_)::value;
        rawbar();
        sb = sb == 65536u ? 0u : sb + 16384u;
#pragma unroll
        for (int ks = 0; ks < 4; ks++) {
          SB0; acc = __builtin_amdgcn_mfma_f32_32x32x16_bf16(FW[P][ks], FH[P][ks], acc, 0, 0, 0); SB0;
          rdf2(IC<1 - P>{}, ks);
        }
      };
      bar();
      rawbar();
#pragma unroll
      for (int ks = 0; ks < 4; ks++) rdf2(IC<0>{}, ks);
#pragma unroll 1
      for (int s = 0; s < 26; s += 2) { step2(IC<0>{}); step2(IC<1>{}); }
      step2(IC<0>{});
#pragma unroll
      for (int ks = 0; ks < 4; ks++) acc = __builtin_amdgcn_mfma_f32_32x32x16_bf16(FW[1][ks], FH[1][ks], acc, 0, 0, 0);
      rawbar();
      const u32 LA_YT = 65536u, LA_OUT = 73728u;
      u32 ya = LA_YT + (u32)((dl + q3) * 128 + (p3 >> 1) * 64 + (wt2 * 2 + blk3) * 16 + (p3 & 1) * 8);
#pragma unroll
      for (int hf = 0; hf < 2; hf++) {
        u32x2 y0 = ldstr(ya + hf * 1024), y1 = ldstr(ya + hf * 1024 + 512);
        u32x4 yy = {y0[0], y0[1], y1[0], y1[1]};
        const u32 oa = LA_OUT + (u32)(tl * 128 + (((4 * wd + 2 * h3 + hf) ^ (tl & 7)) << 4));
        u32x4 xv = ldsld<u32x4>(oa);
        u32x4 ov;
#pragma unroll
        for (int j = 0; j < 4; j++) {
          u32 s1 = pkbf(bf2f(xv[j]) + bf2f(yy[j]), bfhi(xv[j]) + bfhi(yy[j]));
          u32 s2 = pkbf(acc[8 * hf + 2 * j], acc[8 * hf + 2 * j + 1]);
          ov[j] = pkbf(bf2f(s1) + bf2f(s2), bfhi(s1) + bfhi(s2));
        }
        ldsst<u32x4>(oa, ov);
      }
#pragma unroll
      for (int j = 0; j < 2; j++) {
        int t = wt2 * 32 + (l3 >> 2) + 16 * j, c = 4 * wd + (l3 & 3);
        u32x4 ov = ldsld<u32x4>(LA_OUT + (u32)(t * 128 + ((c ^ (t & 7)) << 4)));
        st16((char*)out + (size_t)bm2 * 65536 + bn2 * 128, (u32)(t * 1024 + c * 16), ov);
      }
    } else {
      __builtin_amdgcn_s_setprio(2);
      const int lw = w - 4;
      const bool shk = lw == 3;
      const u32 rot2 = (u32)((bi * 9) % 28), rot1 = (u32)(2 * ((2 * bm + bn) & 7));
      u32 v2, vX, vW;
      {
        const int row2 = 16 * lw + (lane >> 3);
        v2 = (u32)(row2 * 1024 + (((lane & 7) ^ ((row2 & 2) << 1)) << 4) + bn2 * 128);
        int row = 32 * lw + (lane >> 2);
        vX = (u32)((bm * 128 + row) * 1024 + (((lane & 3) ^ ((row >> 2) & 3)) << 4));
        int k = 8 * lw + (lane >> 4);
        vW = (u32)(k * 3584 + (((lane & 15) ^ (((k & 1) << 1) | ((k & 2) << 2))) << 4) + bn * 256);
      }
      const u32x4 sW2 = mksrd(w2, 1792u * 1024u), sX = mksrd(x, 2048u * 1024u), sW1 = mksrd(w1, 512u * 3584u);
      u32 so2 = rot2 * 65536u, soX = rot1 * 64u, soW = rot1 * 114688u, sl = (u32)(lw * 2048);
      const u32 slend = 81920u + (u32)(lw * 2048);
      auto nextsl = [&]() { sl += 16384u; sl = sl == slend ? sl - 81920u : sl; };
      auto next2 = [&]() { so2 += 65536u; so2 = so2 == 28u * 65536u ? 0u : so2; };
      auto i1 = [&]() {
        dma4<false>(sX, soX, vX, vX + 15360u, sW1, soW, vW, vW + 13312u, sl);
        soX = (soX + 64u) & 1023u;
        soW += 114688u; soW = soW == 16u * 114688u ? 0u : soW;
        nextsl();
      };
      auto iw2 = [&]() { dma2<false>(sW2, so2, v2, v2 + 7168u, sl + 8192u); next2(); nextsl(); };
      const char* sb_ = ws + WS_STAT + bm * 1024;
      const char* cb_ = ws + WS_CS + bn * 1024;
#define HLP_POLL() asm volatile("global_load_dwordx4 v[116:119], %0, %1 sc1\n\tglobal_load_dwordx4 v[120:123], %0, %2 sc1" \
                                ::"v"((u32)lane * 16), "s"(sb_), "s"(cb_) : "memory", "v116", "v117", "v118", "v119", "v120", "v121", "v122", "v123")
      if (isg1) {
        if (shk) {
          SHK_POLL();
          asm volatile("global_load_dword v124, %0, %1" ::"v"((u32)lane * 4), "s"(b1 + bn * 128) : "memory", "v124");
        }
#pragma unroll 1
        for (int q = 0; q < 5; q++) i1();
        waitv<16>(); rawbar();
        if (shk) {
          bool asked = false;
          shk_first();
          waitv<12>(); rawbar();
#pragma unroll 1
          for (int s = 1; s <= 14; s++) {
            if (s >= 13) waitv<4>(); else waitv<8>();
            rawbar();
            if (s <= 11) i1(); else iw2();
            if (!rel) { if (PMASK >> s & 1) shk_step(); }
            else if (!asked) { HLP_POLL(); asked = true; }
          }
          shk_finish();
          u32 spins = 0;
          for (;;) {
            if (!asked) HLP_POLL();
            asked = false;
            waitv<0>();
            u64 m0_, m1_, m2_, m3_;
            asm volatile("v_cmp_ne_u32 %0, 1, v117\n\tv_cmp_ne_u32 %1, 1, v119\n\tv_cmp_ne_u32 %2, 1, v121\n\tv_cmp_ne_u32 %3, 1, v123"
                         : "=&s"(m0_), "=&s"(m1_), "=&s"(m2_), "=&s"(m3_)::"memory");
            if ((m0_ | m1_ | m2_ | m3_) == 0 || ++spins > SPIN_MAX) break;
          }
          u32 g0, g1, c0, c1, bv;
          asm volatile("v_mov_b32 %0, v116\n\tv_mov_b32 %1, v118\n\tv_mov_b32 %2, v120\n\tv_mov_b32 %3, v122\n\tv_mov_b32 %4, v124"
                       : "=&v"(g0), "=&v"(g1), "=&v"(c0), "=&v"(c1), "=&v"(bv)::"memory");
          u32 n0 = (g0 & 0xffffu) ^ 0x8000u, n1 = (g1 & 0xffffu) ^ 0x8000u;
          ldsst<u32x4>(LA_XA + lane * 16, (u32x4){n0 | (n0 << 16), g0 >> 16, n1 | (n1 << 16), g1 >> 16});
          ldsst<f32x2>(LA_RS + lane * 8, (f32x2){1.f / bfhi(g0), 1.f / bfhi(g1)});
          float f0 = __uint_as_float(c0), f1 = __uint_as_float(c1), h0 = rbf(f0), h1 = rbf(f1);
          ldsst<u32x4>(LA_WA + lane * 16, (u32x4){pkbf(h0, f0 - h0), bv & 0xffffu, pkbf(h1, f1 - h1), bv >> 16});
          if (lane < 2) ldsst<u32>(LA_ZERO + lane * 4, 0u);
        } else {
          waitv<12>(); rawbar();
#pragma unroll 1
          for (int s = 1; s <= 11; s++) { waitv<8>(); rawbar(); i1(); }
          waitv<8>(); rawbar(); iw2(); waitv<6>(); rawbar(); iw2(); waitv<4>(); rawbar(); iw2();
        }
        bar();
        iw2(); iw2();
        rawbar();
      } else {
        if (shk) {
          if (ci < 28) st4c(ws, WS_CS + (u32)(ci * 512 + lane * 8 + 4), 0u);
          SHK_POLL();
        }
        if (ci < 28) {
          const u32 vc = (u32)((16 * lw + (lane >> 3)) * 3584 + (lane & 7) * 16 + ci * 128);
#pragma unroll 1
          for (int j = 0; j < 8; j++) dma2<false>(sW1, (u32)j * 64u * 3584u, vc, vc + 8u * 3584u - 1024u, (u32)(j * 8192 + lw * 2048));
        }
        waitv<0>();
        rawbar();
        if (shk) shk_first();
        rawbar();
        sl += 16384u;
#pragma unroll 1
        for (int q = 0; q < 5; q++) iw2();
        if (shk) shk_finish();
      }
      if (shk) {
        u32 spins = 0;
        for (;;) {
          u32 f = ld4c_wait(ws + WS_HFLAG + bm2 * 128, (u32)(lane < 28 ? lane : 0) * 4);
          if (__all(f == EP) || ++spins > SPIN_MAX) break;
          __builtin_amdgcn_s_sleep(8);
        }
      }
      const u32x4 sH = mksrd(ws + WS_H + (size_t)bm2 * 229376, 229376u);
      const u32 vH = (u32)(lw * 2048 + lane * 16);
      u32 soH = rot2 * 8192u;
      auto nextH = [&]() { soH += 8192u; soH = soH == 229376u ? 0u : soH; };
      auto i2 = [&]() { dma4<true>(sH, soH, vH, vH, sW2, so2, v2, v2 + 7168u, sl); nextH(); next2(); nextsl(); };
      rawbar();
#pragma unroll 1
      for (int q = 0; q < 5; q++) { dma2<true>(sH, soH, vH, vH, sl); nextH(); nextsl(); }
      waitv<8>(); rawbar();
      waitv<6>(); rawbar();
      waitv<4>(); rawbar(); i2();
      waitv<6>(); rawbar(); i2();
      const int ya_ = bm2 >> 2;
      const char* yfb = ws + WS_YFLAG + ((ya_ >> 2) * 512 + bn2 * 64) * 4;
#pragma unroll 1
      for (int s = 3; s < 21; s++) { waitv<8>(); rawbar(); i2(); }
      asm volatile("global_load_dword v126, %0, %1 sc1" ::"v"((u32)lane * 4), "s"(yfb) : "memory", "v126");
#pragma unroll 1
      for (int s = 21; s < 24; s++) { waitv<8>(); rawbar(); i2(); }
      waitv<8>(); rawbar();
      {
        u64 ne;
        asm volatile("v_cmp_ne_u32 %0, 1, v126" : "=s"(ne)::"memory");
        const u32x4 sY = mksrd(ws + WS_Y, 2048u * 1024u);
        const u32 vy = (u32)((bn2 * 64 + 16 * lw + (lane >> 3)) * 4096 + ya_ * 512 + (bm2 & 3) * 64 + ((lane >> 2) & 1) * 256 + (lane & 3) * 16);
        const u32 vx = (u32)((bm2 * 64 + 16 * lw + (lane >> 3)) * 1024 + bn2 * 128 + (((lane & 7) ^ (lane >> 3)) << 4));
        if (ne == 0) {
          dma4<true>(sY, 0u, vy, vy + 32768u - 1024u, sX, 0u, vx, vx + 7168u, 65536u + (u32)lw * 2048u);
          waitv<8>(); rawbar(); waitv<4>(); rawbar();
        } else {
          waitv<4>(); rawbar(); waitv<0>(); rawbar();
          u32 spins = 0, yf;
          do { yf = ld4c_wait(yfb, (u32)lane * 4); } while (!__all(yf == EP) && ++spins < SPIN_MAX);
          dma4<true>(sY, 0u, vy, vy + 32768u - 1024u, sX, 0u, vx, vx + 7168u, 65536u + (u32)lw * 2048u);
        }
      }
      waitv<0>();
      rawbar();
    }
  } else {
    const int G = w >> 2, kch = (w >> 1) & 1, kmh = w & 1;
    const int dch = 2 * tb + kch;
    const bool shk = w == 7;
    __builtin_amdgcn_s_setprio(2);
    int ust = 0;
#define UG_POLL() asm volatile("global_load_dwordx4 v[120:123], %0, %1 sc1" ::"v"((u32)lane * 16), "s"(ws + WS_UFLAG) : "memory", "v120", "v121", "v122", "v123")
    auto bsvc = [&]() {
      if (ust == 0) {
        shk_step();
        if (rel) { asm volatile("v_mov_b32 v120, 0" ::: "v120"); UG_POLL(); ust = 1; }
      } else if (ust == 1) {
        u64 m0_, m1_, m2_, m3_;
        asm volatile("v_cmp_ne_u32 %0, 1, v120\n\tv_cmp_ne_u32 %1, 1, v121\n\tv_cmp_ne_u32 %2, 1, v122\n\tv_cmp_ne_u32 %3, 1, v123"
                     : "=&s"(m0_), "=&s"(m1_), "=&s"(m2_), "=&s"(m3_)::"memory");
        if ((m0_ | m1_ | m2_ | m3_) == 0) {
          u32 vo = (u32)lane * 8192;
#pragma unroll
          for (int j = 0; j < 4; j++)
            glds2<4608, true>(ws + WS_U + (size_t)tb * 32 + (size_t)j * 524288, vo, vo + 16, L_UL + 512 + (u32)j * 1024);
          ust = 2;
        } else UG_POLL();
      }
    };
    u32x4 xr;
    u32 pa, pb, pc_, pd, pe, pf, pl, pk_;
    {
      u32 vo = (u32)((8 * tb + w) * 1024 + lane * 16);
      u32 po = (u32)((dch * 128 + kmh * 64 + lane) * 2);
      u32 lo = (u32)(dch * 2);
      asm volatile(
          "global_load_dwordx4 %0, %9, %12\n\t"
          "global_load_ushort %1, %10, %13\n\t"
          "global_load_ushort %2, %10, %14\n\t"
          "global_load_ushort %3, %10, %15\n\t"
          "global_load_ushort %4, %10, %16\n\t"
          "global_load_ushort %5, %10, %17\n\t"
          "global_load_ushort %6, %10, %18\n\t"
          "global_load_ushort %7, %11, %19\n\t"
          "global_load_ushort %8, %11, %20"
          : "=&v"(xr), "=&v"(pa), "=&v"(pb), "=&v"(pc_), "=&v"(pd), "=&v"(pe), "=&v"(pf), "=&v"(pl), "=&v"(pk_)
          : "v"(vo), "v"(po), "v"(lo), "s"(x), "s"(Are), "s"(Aim), "s"(Bre), "s"(Bim), "s"(Cre), "s"(Cim), "s"(ldt), "s"(Dsk)
          : "memory");
    }
    if (shk) {
      const u32 os = WS_STAT + (u32)(8 * tb + lane) * 8 + 4;
      const u32 oy = WS_YFLAG + (u32)((lane & 1) * 512 + 2 * tb + ((lane >> 1) & 1)) * 4;
      const u32 oh = WS_HFLAG + (u32)((bm * 2 + ((lane >> 1) & 1)) * 32 + bn * 2 + (lane & 1)) * 4;
      u32 off = lane < 8 ? os : (lane < 12 ? oy : ((lane < 16 && isg1) ? oh : WS_UFLAG + (u32)tb * 4));
      if (lane < 17) st4c(ws, off, 0u);
      SHK_POLL();
    }
    const int ech = tid >> 8, eg = tid & 255;
    const u32 pb0 = L_TAB + (u32)ech * 16384;
    u32 ad[12];
    {
      const float lf = (float)(2047 - 8 * eg);
      const u32 cb0 = pb0 - 0x3f00u * 4u;
#pragma unroll
      for (int e = 0; e < 12; e += 2) {
        u32 pk2 = pkbf(fmaxf(lf - (float)e, 0.5f), fmaxf(lf - (float)(e + 1), 0.5f));
        ad[e] = cb0 + ((pk2 & 0xffffu) << 2);
        ad[e + 1] = cb0 + ((pk2 >> 16) << 2);
        asm volatile("" : "+v"(ad[e]), "+v"(ad[e + 1]));
      }
    }
    if (shk) waitv<2>(); else waitv<0>();
    asm volatile("" : "+v"(xr), "+v"(pa), "+v"(pb), "+v"(pc_), "+v"(pd), "+v"(pe), "+v"(pf), "+v"(pl), "+v"(pk_)::"memory");
    {
      float v[8];
#pragma unroll
      for (int j = 0; j < 4; j++) { v[2 * j] = bf2f(xr[j]); v[2 * j + 1] = bfhi(xr[j]); }
      float mean = wave_sum(((v[0] + v[1]) + (v[2] + v[3])) + ((v[4] + v[5]) + (v[6] + v[7]))) * (1.f / 512.f);
      float q = 0.f;
#pragma unroll
      for (int j = 0; j < 8; j++) { float d = v[j] - mean; q = __builtin_fmaf(d, d, q); }
      float var = wave_sum(q) * (1.f / 512.f);
      float mb = rbf(mean), sd = rbf(__builtin_sqrtf(rbf(rbf(var) + 1e-5f)));
      float rs = __builtin_amdgcn_rcpf(sd);
      u32x4 o;
#pragma unroll
      for (int j = 0; j < 4; j++) {
        u32 dd = pkbf(v[2 * j] - mb, v[2 * j + 1] - mb);
        float d0 = bf2f(dd), d1 = bfhi(dd);
        float q0 = d0 * rs, q1 = d1 * rs;
        q0 = __builtin_fmaf(__builtin_fmaf(-q0, sd, d0), rs, q0);
        q1 = __builtin_fmaf(__builtin_fmaf(-q1, sd, d1), rs, q1);
        u32 qq = pkbf(q0, q1);
        o[j] = pkh(bf2f(qq), bfhi(qq));
      }
      ldsst<u32x4>(L_LNS + w * 1088 + lane * 16, o);
      if (lane == 0) ldsst<u32>(L_ST + w * 4, (__float_as_uint(mb) >> 16) | (__float_as_uint(sd) & 0xffff0000u));
    }
    if (shk) { waitv<0>(); shk_first(); }
    bar();
    if (shk) {
      const int li = lane & 15;
      u32 a = L_LNS + (li >> 2) * 1088 + ((lane & 48) + 4 * (li & 3)) * 2;
#pragma unroll
      for (int j = 0; j < 8; j++) {
        u32x2 p0 = ldstr(a + j * 128), p1 = ldstr(a + j * 128 + 4 * 1088);
        st16c(ws + WS_U + (size_t)tb * 8192, (u32)(j * 1024 + lane * 16), (u32x4){p0[0], p0[1], p1[0], p1[1]});
      }
      u32 pay = ldsld<u32>(L_ST + (lane & 7) * 4);
      if (lane < 8) st8c(ws, WS_STAT + (u32)(8 * tb + lane) * 8, (u32x2){pay, EP});
      bsvc();
    }
    const int rr = lane & 15, kg = lane >> 4;
    {
      float dt = rbf(expf(bf2f(pl)));
      float a2 = (dt * bf2f(pa)) * 1.44269504088896341f;
      float bb = dt * bf2f(pb);
      f32x2 W = cmul((f32x2){bf2f(pe), bf2f(pf)}, (f32x2){dt * bf2f(pc_), dt * bf2f(pd)});
      const float C_HI = 0.15915494309189535f, C_LO = (float)(0.15915494309189533577 - (double)0.15915494309189535f);
      float p = bb * C_HI;
      float er = __builtin_fmaf(bb, C_HI, -p);
      float fhi = __uint_as_float(__float_as_uint(p) & 0xfffff000u);
      float flo = (p - fhi) + __builtin_fmaf(bb, C_LO, er);
      float g = 0.f;
      if (__any(a2 > 0.f)) { g = wave_max0(fmaxf(a2, 0.f)); a2 -= g; }
      float wsum = wave_sum(fabsf(W[0]) + fabsf(W[1]));
      float sig_, isig;
      pow2scale(wsum, 13, sig_, isig);
      const f32x2 W0 = {W[0] * sig_, -W[1] * sig_};
      const u32 T = L_TAB + w * 8192;
      const u32 wbase = T + (lane >> 2) * 16 + (lane & 3) * 4;
      const u32 ra = T + rr * 256 + ((kg ^ rr) << 4);
      const float cs = G ? 8.f : 2.f, rs = G ? 128.f : 32.f, rb = G ? 512.f : 0.f;
      f32x2 zc = cpow(a2, -fhi, -flo, cs), zr = cpow(a2, fhi, flo, rs);
      f32x2 zc4 = cpow(a2, -fhi, -flo, 4.f * cs), zr4 = cpow(a2, fhi, flo, 4.f * rs);
      f32x2 zb = cpow(a2, fhi, flo, rb), zx = cpow(a2, fhi, flo, 0.5f * cs);
      f32x2 zc2 = cmul(zc, zc), zr2 = cmul(zr, zr);
      f32x2 c[4], r[4], e0[4], e1[4];
      c[0] = W0; c[1] = cmul(W0, zc); r[0] = zb; r[1] = cmul(zb, zr);
      c[2] = cmul(c[0], zc2); c[3] = cmul(c[1], zc2); r[2] = cmul(r[0], zr2); r[3] = cmul(r[1], zr2);
      SB0;
      if (shk) { waitv<0>(); if (lane == 0) st4c(ws, WS_UFLAG + (u32)tb * 4, EP); bsvc(); }
      SB0;
#pragma unroll
      for (int o = 0; o < 4; o++) {
        if (o > 0) { cmul4(c[0], c[1], c[2], c[3], zc4); cmul4(r[0], r[1], r[2], r[3], zr4); }
        if (o == 0) { e0[0] = r[0]; e0[1] = r[1]; e0[2] = r[2]; e0[3] = r[3]; cmul4(e0[0], e0[1], e0[2], e0[3], zx); }
        if (o == 1) { e1[0] = r[0]; e1[1] = r[1]; e1[2] = r[2]; e1[3] = r[3]; cmul4(e1[0], e1[1], e1[2], e1[3], zx); }
#pragma unroll
        for (int j = 0; j < 4; j++) {
          int e = 4 * o + j;
          u32 a = wbase ^ (u32)(e << 4);
          ldsst<u32>(a + e * 256, pkh(r[j][0], r[j][1]));
          ldsst<u32>(a + e * 256 + 4096, pkh(c[j][0], c[j][1]));
        }
        if (shk) bsvc();
      }
      f16x8 af[4], bf[4];
#pragma unroll
      for (int s = 0; s < 4; s++) { af[s] = ldsld<f16x8>(ra ^ (u32)(s << 6)); bf[s] = ldsld<f16x8>((ra ^ (u32)(s << 6)) + 4096); }
#pragma unroll
      for (int j = 0; j < 4; j++) {
        ldsst<u32>((wbase ^ (u32)(j << 4)) + j * 256, pkh(e0[j][0], e0[j][1]));
        ldsst<u32>((wbase ^ (u32)((4 + j) << 4)) + (4 + j) * 256, pkh(e1[j][0], e1[j][1]));
      }
      f16x8 ag[4];
#pragma unroll
      for (int s = 0; s < 4; s++) ag[s] = ldsld<f16x8>(ra ^ (u32)(s << 6));
      if (shk) bsvc();
      bar();
      if (shk) bsvc();
      f32x4 d0 = {0.f, 0.f, 0.f, 0.f}, d1 = {0.f, 0.f, 0.f, 0.f};
#pragma unroll
      for (int s = 0; s < 4; s++) d0 = __builtin_amdgcn_mfma_f32_16x16x32_f16(af[s], bf[s], d0, 0, 0, 0);
#pragma unroll
      for (int s = 0; s < 4; s++) d1 = __builtin_amdgcn_mfma_f32_16x16x32_f16(ag[s], bf[s], d1, 0, 0, 0);
      const u32 PB = L_TAB + (u32)(w & 3) * 8192 - 0x3f00u * 4u;
      const float l00 = rb + rs * (float)(4 * kg) + cs * (float)rr;
#pragma unroll
      for (int e = 0; e < 4; e++) {
        float l0 = l00 + rs * (float)e, l1 = l0 + 0.5f * cs;
        float s0 = isig, s1 = isig;
        if (g > 0.f) { s0 *= __builtin_amdgcn_exp2f(g * l0); s1 *= __builtin_amdgcn_exp2f(g * l1); }
        l0 = fmaxf(l0, 0.5f);
        l0 = l0 > 2048.f ? 4096.f : l0;
        l1 = l1 < 128.f * cs ? l1 : 4096.f;
        ldsst<float>(PB + ((__float_as_uint(l0) >> 16) << 2), d0[e] * s0);
        ldsst<float>(PB + ((__float_as_uint(l1) >> 16) << 2), d1[e] * s1);
      }
      if (lane == 0 && G == 0) ldsst<float>(T + 7168, wsum * __builtin_amdgcn_exp2f(g * 2048.f));
    }
    if (shk) bsvc();
    bar();
    if (shk) bsvc();
    float kinv;
    {
      float bnd = ldsld<float>(pb0 + 7168) + ldsld<float>(pb0 + 8192 + 7168);
      float bnd2 = ldsld<float>(L_TAB + kch * 16384 + 7168) + ldsld<float>(L_TAB + kch * 16384 + 8192 + 7168);
      float v[11];
#pragma unroll
      for (int e = 0; e < 11; e++) v[e] = ldsld<float>(ad[e]) + ldsld<float>(ad[e] + 8192);
      float ks, ki, ks2;
      pow2scale(bnd, 14, ks, ki);
      pow2scale(bnd2, 14, ks2, kinv);
      u32 PK[5], QK[5];
#pragma unroll
      for (int e = 0; e < 11; e++) v[e] *= (e >= 8 && eg == 255) ? 0.f : ks;
#pragma unroll
      for (int j = 0; j < 5; j++) { PK[j] = pkh(v[2 * j], v[2 * j + 1]); QK[j] = pkh(v[2 * j + 1], v[2 * j + 2]); }
      u32 a = L_KCP + ech * 16640 + eg * 16;
      ldsst<u32x4>(a, (u32x4){PK[0], PK[1], PK[2], PK[3]});
      ldsst<u32x4>(a + 4160, (u32x4){QK[0], QK[1], QK[2], QK[3]});
      ldsst<u32x4>(a + 8320, (u32x4){PK[1], PK[2], PK[3], PK[4]});
      ldsst<u32x4>(a + 12480, (u32x4){QK[1], QK[2], QK[3], QK[4]});
      if (tid < 16) ldsst<u32x4>(L_KCP + (tid >> 1) * 4160 + (256 + (tid & 1)) * 16, (u32x4){0, 0, 0, 0});
      else if (tid >= 64 && tid < 128) ldsst<u32x4>(L_UL + ((tid >> 5) & 1) * 4608 + (tid & 31) * 16, (u32x4){0, 0, 0, 0});
    }
    if (shk) {
      u32 spins = 0;
      while (ust < 2 && ++spins < SPIN_MAX) { waitv<0>(); bsvc(); }
      waitv<0>();
    }
    __builtin_amdgcn_s_setprio(0);
    bar();
    if (w < 4) {
      const int n = rr;
      f32x4 acc[8];
#pragma unroll
      for (int a = 0; a < 8; a++) acc[a] = (f32x4){0.f, 0.f, 0.f, 0.f};
      const int cc = (3 - n) & 3;
      u32 ub = L_UL + kch * 4608 + (u32)((256 + 16 * (n - 8 * kmh) - 16 + 8 * kg) * 2);
      u32 ab = L_KCP + kch * 16640 + cc * 4160 + (u32)((2031 - n + 8 * kg - cc - 1792 - 128 * kmh) * 2);
      f16x8 Bq[8];
      u32x2 Al[4], Ah[4];
#pragma unroll
      for (int q = 0; q < 8; q++) Bq[q] = ldsld<f16x8>(ub + q * 512);
#pragma unroll
      for (int i = 0; i < 4; i++) { Al[i] = ldsldv<u32x2>(ab + (7 - i) * 512); Ah[i] = ldsldv<u32x2>(ab + (7 - i) * 512 + 8); }
#pragma unroll 1
      for (int it = 0; it < 4; it++) {
#pragma unroll
        for (int i = 0; i < 8; i++) {
          const int in = (i + 4) & 7;
          u32 a = ab + (7 - in) * 512 - (i >= 4 ? 64 : 0);
          f16x8 A = __builtin_bit_cast(f16x8, (u32x4){Al[i & 3][0], Al[i & 3][1], Ah[i & 3][0], Ah[i & 3][1]});
#pragma unroll
          for (int a2 = 7; a2 >= i; a2--) acc[a2] = __builtin_amdgcn_mfma_f32_16x16x32_f16(A, Bq[a2 - i], acc[a2], 0, 0, 0);
          SB0;
          Al[i & 3] = ldsldv<u32x2>(a); Ah[i & 3] = ldsldv<u32x2>(a + 8);
          Bq[7 - i] = ldsld<f16x8>(ub - 64 + (7 - i) * 512);
          SB0;
        }
        ub -= 64; ab -= 64;
      }
      if (kmh) {
#pragma unroll
        for (int a = 0; a < 4; a++) { f32x4 t = acc[a]; acc[a] = acc[4 + a]; acc[4 + a] = t; asm volatile("" : "+v"(acc[a]), "+v"(acc[4 + a])); }
      }
#pragma unroll
      for (int a = 0; a < 4; a++) ldsst<f32x4>(L_RED + (u32)(w * 4096 + a * 1024 + lane * 16), acc[4 + a]);
      bar();
      const float dsk = bf2f(pk_);
      char* yp = ws + WS_Y + (size_t)dch * 4096 + kmh * 2048;
#pragma unroll
      for (int pr = 0; pr < 2; pr++) {
        u32 pk[2][2];
#pragma unroll
        for (int j = 0; j < 2; j++) {
          const int a = 2 * pr + j;
          f32x4 yy = acc[a] + ldsld<f32x4>(L_RED + (u32)((w ^ 1) * 4096 + a * 1024 + lane * 16));
          f16x4 uu = ldsld<f16x4>(L_UL + kch * 4608 + 512 + (u32)((256 * (4 * kmh + a) + 16 * n + 4 * kg) * 2));
          u32 y01 = pkbf(yy[0] * kinv, yy[1] * kinv), y23 = pkbf(yy[2] * kinv, yy[3] * kinv);
          u32 s01 = pkbf((float)uu[0] * dsk, (float)uu[1] * dsk), s23 = pkbf((float)uu[2] * dsk, (float)uu[3] * dsk);
          pk[j][0] = pkbf(bf2f(y01) + bf2f(s01), bfhi(y01) + bfhi(s01));
          pk[j][1] = pkbf(bf2f(y23) + bf2f(s23), bfhi(y23) + bfhi(s23));
        }
        auto s0 = __builtin_amdgcn_permlane16_swap(pk[0][0], pk[1][0], false, false);
        auto s1 = __builtin_amdgcn_permlane16_swap(pk[0][1], pk[1][1], false, false);
        st16c(yp, (u32)((2 * pr + (kg & 1)) * 512 + (kg >> 1) * 256 + n * 16), (u32x4){s0[0], s1[0], s0[1], s1[1]});
      }
      waitv<0>();
      if (lane == 0) st4c(ws, WS_YFLAG + (u32)(kmh * 512 + dch) * 4, EP);
    }
  }
}

extern "C" void kernel_launch(void* const* d_in, const int* in_sizes, int n_in, void* d_out, int out_size, void* d_ws,
                              size_t ws_size, hipStream_t stream) {
  fused_kernel<<<512, 512, 0, stream>>>((const u16*)d_in[0], (const u16*)d_in[1], (const u16*)d_in[2], (const u16*)d_in[3],
                                        (const u16*)d_in[4], (const u16*)d_in[5], (const u16*)d_in[6], (const u16*)d_in[7],
                                        (const u16*)d_in[8], (const u16*)d_in[10], (const u16*)d_in[11], (const u16*)d_in[12],
                                        (u16*)d_out, (char*)d_ws);
}
```
